```python
import torch

def _softmax(x, axis=-1):
    return torch.softmax(x, dim=axis)

def _gelu(x):
    return torch.nn.functional.gelu(x, approximate='tanh')

def _silu(x):
    return torch.nn.functional.silu(x)

def _sigmoid(x):
    return torch.sigmoid(x)

def _layernorm(x, axis=-1, eps=1e-05):
    m = x.mean(dim=axis, keepdim=True)
    v = x.var(dim=axis, keepdim=True, unbiased=False)
    return (x - m) / torch.sqrt(v + eps)

def _scatter_add(tgt, idx, src, axis=0):
    return tgt.clone().index_add_(axis, idx, src)

def _segment_sum(src, seg, n):
    out = torch.zeros((n,) + src.shape[1:], dtype=src.dtype)
    return out.index_add_(0, seg, src)

def _pad(x, pw):
    flat = tuple((v for pair in reversed(pw) for v in pair))
    return torch.nn.functional.pad(x, flat)
kernel_name = 'adaLN_cond__ssm_conv_s4__glu_gated_glu__prompt'

def setup_inputs():

    _w = lambda a: (lambda t: t.to(torch.bfloat16) if t.is_floating_point() else t)(torch.as_tensor(a))

    def _A_setup():
        B, L, D, N = (1, 2048, 512, 128)
        A_re = -(lambda _x: torch.exp(torch.as_tensor(_x)))((lambda *s: torch.randn(*s, dtype=torch.bfloat16) * 3.2)(D, N))
        A_im = torch.pi * (lambda *s: torch.randn(*s, dtype=torch.bfloat16) * 0.1)(D, N)
        B_re = (lambda *s: torch.randn(*s, dtype=torch.bfloat16) * 0.2)(D, N)
        B_im = (lambda *s: torch.randn(*s, dtype=torch.bfloat16) * 0.2)(D, N)
        C_re = (lambda *s: torch.randn(*s, dtype=torch.bfloat16) * 0.2)(D, N)
        C_im = (lambda *s: torch.randn(*s, dtype=torch.bfloat16) * 0.2)(D, N)
        log_dt = (lambda *s: torch.randn(*s, dtype=torch.bfloat16) * 0.1)(D) * 0.5 - 4.0
        Dskip = (lambda *s: torch.randn(*s, dtype=torch.bfloat16) * 0.1)(D)
        return {'u': _w((lambda *s: torch.randn(*s, dtype=torch.bfloat16) * 0.05)(B, L, D)), 'A_re': _w(A_re), 'A_im': _w(A_im), 'B_re': _w(B_re), 'B_im': _w(B_im), 'C_re': _w(C_re), 'C_im': _w(C_im), 'log_dt': _w(log_dt), 'Dskip': _w(Dskip)}
    _A_in = _A_setup()

    def _B_setup():
        B, S, D, F = (1, 2048, 512, 1792)
        return {'x': _w((lambda *s: torch.randn(*s, dtype=torch.bfloat16) * 0.05)(B, S, D)), 'w_gate': _w((lambda *s: torch.randn(*s, dtype=torch.bfloat16) * 0.05)(D, F)), 'w_up': _w((lambda *s: torch.randn(*s, dtype=torch.bfloat16) * 0.05)(D, F)), 'w_down': _w((lambda *s: torch.randn(*s, dtype=torch.bfloat16) * 0.05)(F, D))}
    _B_in = _B_setup()
    return {'A_u': _A_in['u'], 'A_A_re': _A_in['A_re'], 'A_A_im': _A_in['A_im'], 'A_B_re': _A_in['B_re'], 'A_B_im': _A_in['B_im'], 'A_C_re': _A_in['C_re'], 'A_C_im': _A_in['C_im'], 'A_log_dt': _A_in['log_dt'], 'A_Dskip': _A_in['Dskip'], 'B_x': _B_in['x'], 'B_w_gate': _B_in['w_gate'], 'B_w_up': _B_in['w_up'], 'B_w_down': _B_in['w_down'], 'c': _w((lambda *s: torch.randn(*s, dtype=torch.bfloat16) * 0.1)(1, 512) * 8.0), 'w_mod': _w((lambda *s: torch.randn(*s, dtype=torch.bfloat16) * 0.1)(512, 6 * 512) * 512 ** (-0.5) * 8.0)}

def reference(A_u, A_A_re, A_A_im, A_B_re, A_B_im, A_C_re, A_C_im, A_log_dt, A_Dskip, B_x, B_w_gate, B_w_up, B_w_down, c, w_mod):

    def _A_ref(u, A_re, A_im, B_re, B_im, C_re, C_im, log_dt, Dskip):
        Bsz, L, D = u.shape
        dt = torch.exp(log_dt)[:, None]
        A = A_re + 1j * A_im
        Bc = B_re + 1j * B_im
        Cc = C_re + 1j * C_im
        logdA = dt * A
        dB = dt * Bc
        l = (lambda x, dt: x.to(dt))(torch.arange(L), A_re.dtype)
        P = torch.exp(logdA[:, :, None] * l[None, None, :])
        K = torch.einsum('dn,dnl,dn->dl', Cc, P, dB).real
        L2 = 2 * L
        Kp = _pad(K, ((0, 0), (0, L)))
        up = (lambda x, *d: x.permute(*d))(u, 0, 2, 1)
        up = _pad(up, ((0, 0), (0, 0), (0, L)))
        Kf = (lambda x, axes=(-2, -1): torch.fft.rfft2(x.float(), dim=axes))(Kp[:, None, :], axes=(-2, -1))
        Uf = (lambda x, axes=(-2, -1): torch.fft.rfft2(x.float(), dim=axes))(up[:, :, None, :], axes=(-2, -1))
        Yf = Uf * Kf[None, :, :, :]
        y = (lambda x, s, axes=(-2, -1): torch.fft.irfft2(x, s=s, dim=axes).to(torch.bfloat16))(Yf, (1, L2), axes=(-2, -1))[:, :, 0, :L]
        y = (lambda x, *d: x.permute(*d))(y, 0, 2, 1) + u * Dskip[None, None, :]
        return y

    def _B_ref(x, w_gate, w_up, w_down):
        g = torch.einsum('bsd,df->bsf', x, w_gate)
        u = torch.einsum('bsd,df->bsf', x, w_up)
        h = _sigmoid(g) * u
        return torch.einsum('bsf,fd->bsd', h, w_down)
    x = A_u
    mod = (_silu(c) @ w_mod).reshape(c.shape[0], 6, 1, -1)
    sa, ca, ga, sb, cb, gb = (mod[:, 0].clone(), mod[:, 1].clone(), mod[:, 2].clone(), mod[:, 3].clone(), mod[:, 4].clone(), mod[:, 5].clone())
    h = _layernorm(x, axis=-1) * (1 + ca) + sa
    x = x + ga * _A_ref(h, A_A_re, A_A_im, A_B_re, A_B_im, A_C_re, A_C_im, A_log_dt, A_Dskip)
    h = _layernorm(x, axis=-1) * (1 + cb) + sb
    x = x + gb * _B_ref(h, B_w_gate, B_w_up, B_w_down)
    return x
```

```cpp
#include <hip/hip_runtime.h>
#include <hip/hip_bf16.h>
#include <stdint.h>
typedef unsigned short u16;
__device__ __forceinline__ float bf2f(u16 v){ return __uint_as_float(((unsigned)v)<<16); }
__device__ __forceinline__ u16 f2bf(float f){ __hip_bfloat16 b = __float2bfloat16(f); return *(u16*)&b; }
__device__ __forceinline__ float rbf(float f){ return bf2f(f2bf(f)); }
constexpr int L=2048, D=512, N=128, F=1792;

__global__ void k_mod(const u16* c, const u16* wmod, float* mod){
  int j = blockIdx.x*blockDim.x+threadIdx.x; if(j>=6*D) return;
  float acc=0;
  for(int k=0;k<D;k++){ float cv=bf2f(c[k]); float s=rbf(cv/(1.f+expf(-cv))); acc+=s*bf2f(wmod[k*6*D+j]); }
  mod[j]=rbf(acc);
}
__global__ void k_ln(const u16* x, const float* mod, int soff, int coff, u16* h){
  __shared__ float red[512];
  int t=blockIdx.x, d=threadIdx.x;
  float v=bf2f(x[t*D+d]);
  red[d]=v; __syncthreads();
  for(int s=256;s>0;s>>=1){ if(d<s) red[d]+=red[d+s]; __syncthreads(); }
  float mf=red[0]/D; __syncthreads();
  float dv=v-mf; red[d]=dv*dv; __syncthreads();
  for(int s=256;s>0;s>>=1){ if(d<s) red[d]+=red[d+s]; __syncthreads(); }
  float var=rbf(red[0]/D); float m=rbf(mf);
  float den=rbf(sqrtf(rbf(var+1e-5f)));
  float xn=rbf(rbf(v-m)/den);
  float hv=rbf(rbf(xn*rbf(1.f+mod[coff+d]))+mod[soff+d]);
  h[t*D+d]=f2bf(hv);
}
__global__ void k_kgen(const u16* Are,const u16* Aim,const u16* Bre,const u16* Bim,const u16* Cre,const u16* Cim,const u16* logdt,float* K){
  int d=blockIdx.x;
  float dt=rbf(expf(bf2f(logdt[d])));
  for(int l=threadIdx.x;l<L;l+=blockDim.x){
    float lf=rbf((float)l); float acc=0;
    for(int n=0;n<N;n++){
      float ar=dt*bf2f(Are[d*N+n]), ai=dt*bf2f(Aim[d*N+n]);
      float br=dt*bf2f(Bre[d*N+n]), bi=dt*bf2f(Bim[d*N+n]);
      float cr=bf2f(Cre[d*N+n]), ci=bf2f(Cim[d*N+n]);
      float e=expf(ar*lf), s, c; sincosf(ai*lf,&s,&c);
      float pr=e*c, pi=e*s;
      float wr=cr*br-ci*bi, wi=cr*bi+ci*br;
      acc+=wr*pr-wi*pi;
    }
    K[d*L+l]=acc;
  }
}
__global__ void k_conv(const u16* x,const u16* h,const float* K,const u16* Dskip,const float* mod,u16* x1){
  int d=blockIdx.x; __shared__ float sk[L], sh[L];
  for(int i=threadIdx.x;i<L;i+=blockDim.x){ sk[i]=K[d*L+i]; sh[i]=bf2f(h[i*D+d]); }
  __syncthreads();
  float ds=bf2f(Dskip[d]), ga=mod[2*D+d];
  for(int t=threadIdx.x;t<L;t+=blockDim.x){
    float acc=0; for(int s=0;s<=t;s++) acc+=sk[t-s]*sh[s];
    float y=rbf(rbf(acc)+rbf(sh[t]*ds));
    x1[t*D+d]=f2bf(bf2f(x[t*D+d])+rbf(ga*y));
  }
}
__global__ void k_g1(const u16* h,const u16* wg,const u16* wu,u16* hh){
  int t=blockIdx.y, f=blockIdx.x*blockDim.x+threadIdx.x;
  float g=0,u=0; for(int k=0;k<D;k++){ float a=bf2f(h[t*D+k]); g+=a*bf2f(wg[k*F+f]); u+=a*bf2f(wu[k*F+f]); }
  g=rbf(g); u=rbf(u); float s=rbf(1.f/(1.f+expf(-g))); hh[t*F+f]=f2bf(s*u);
}
__global__ void k_g2(const u16* hh,const u16* wd,const u16* x1,const float* mod,u16* out){
  int t=blockIdx.y, d=blockIdx.x*blockDim.x+threadIdx.x;
  float a=0; for(int k=0;k<F;k++) a+=bf2f(hh[t*F+k])*bf2f(wd[k*D+d]);
  out[t*D+d]=f2bf(bf2f(x1[t*D+d])+rbf(mod[5*D+d]*rbf(a)));
}
extern "C" void kernel_launch(void* const* d_in, const int* in_sizes, int n_in, void* d_out, int out_size, void* d_ws, size_t ws_size, hipStream_t stream){
  const u16* x=(const u16*)d_in[0];
  char* ws=(char*)d_ws;
  float* mod=(float*)ws; u16* h=(u16*)(ws+(1<<20)); float* K=(float*)(ws+(4<<20)); u16* x1=(u16*)(ws+(12<<20)); u16* h2=(u16*)(ws+(16<<20)); u16* hh=(u16*)(ws+(20<<20));
  k_mod<<<12,256,0,stream>>>((const u16*)d_in[13],(const u16*)d_in[14],mod);
  k_ln<<<L,512,0,stream>>>(x,mod,0,D,h);
  k_kgen<<<D,256,0,stream>>>((const u16*)d_in[1],(const u16*)d_in[2],(const u16*)d_in[3],(const u16*)d_in[4],(const u16*)d_in[5],(const u16*)d_in[6],(const u16*)d_in[7],K);
  k_conv<<<D,256,0,stream>>>(x,h,K,(const u16*)d_in[8],mod,x1);
  k_ln<<<L,512,0,stream>>>(x1,mod,3*D,4*D,h2);
  k_g1<<<dim3(F/256,L),256,0,stream>>>(h2,(const u16*)d_in[10],(const u16*)d_in[11],hh);
  k_g2<<<dim3(D/256,L),256,0,stream>>>(hh,(const u16*)d_in[12],x1,mod,(u16*)d_out);
}
```

```cpp
#include <hip/hip_runtime.h>
#include <stdint.h>

#define DBG 0

namespace s4glu {

typedef unsigned short u16;
typedef unsigned u32;
typedef unsigned long long u64;
typedef u32 v4u __attribute__((ext_vector_type(4)));
typedef u32 v2u __attribute__((ext_vector_type(2)));
typedef float f2 __attribute__((ext_vector_type(2)));
typedef float f4 __attribute__((ext_vector_type(4)));
typedef float f16v __attribute__((ext_vector_type(16)));
typedef __bf16 bf2 __attribute__((ext_vector_type(2)));
typedef __bf16 bf8 __attribute__((ext_vector_type(8)));
typedef _Float16 h2 __attribute__((ext_vector_type(2)));
typedef _Float16 h8 __attribute__((ext_vector_type(8)));
typedef short s4v __attribute__((ext_vector_type(4)));
typedef __amdgpu_buffer_rsrc_t rsrc_t;
#define LAS __attribute__((address_space(3)))
#define CAS __attribute__((address_space(4)))
#define DEV __device__ __forceinline__

constexpr int kL = 2048, kD = 512, kN = 128, kF = 1792;

constexpr u32 WS_NONCE = 0;
constexpr u32 WS_STG = 4096;
constexpr u32 WS_MODG = WS_STG + 16384;
constexpr u32 WS_F2 = WS_MODG + 98304;
constexpr u32 WS_F3 = WS_F2 + 131072;
constexpr u32 WS_XG = 0x80000;
constexpr u32 WS_X1T = WS_XG + 4194304;
constexpr u32 WS_HH = WS_X1T + 2097152;
constexpr u32 WS_END = WS_HH + 7340032;
constexpr u32 WS_DBG = WS_END;
static_assert(WS_F3 + 65536 <= WS_XG, "ws map");

constexpr u32 L_V = 0;
constexpr u32 L_ST = 2048;
constexpr u32 L_STAT = 3840;
constexpr u32 L_GB = 4864;
constexpr u32 L_MISC = 5120;
constexpr u32 L_STAMP = 5376;
constexpr u32 L_X = 6144;
constexpr u32 L_FACT = L_X;
constexpr u32 L_KC = L_X;
constexpr u32 L_HR = L_KC + 33280;
constexpr u32 L_XB = L_HR + 9216;
constexpr u32 L_P = L_XB + 8192;
constexpr u32 L_TA = L_X;
constexpr u32 L_STG = L_X + 67072;
constexpr u32 SLOT = 14464, UPOFF = 7296;
constexpr u32 L_S5 = L_STG + 5 * SLOT;
constexpr u32 L_ROW = L_S5;
constexpr u32 L_MODP = L_ROW + 8320;
constexpr u32 L_G = L_MODP + 2048;
constexpr u32 L_DIAG = L_S5;
constexpr u32 L_TB = L_STG;
constexpr u32 L_END = 163840;
static_assert(L_G + 2704 <= L_STG + 6 * SLOT && L_STG + 6 * SLOT <= L_END, "lds map");
constexpr u32 M_CNT = L_MISC;
constexpr u32 M_RST = L_MISC + 32;
constexpr u32 M_INV = L_MISC + 64;

constexpr u32 kSpin = 40000;

struct Params {
  const void *x, *are, *aim, *bre, *bim, *cre, *cim, *logdt, *dskip, *wg, *wu, *wd, *c, *wmod;
  void *out, *ws;
};

DEV rsrc_t mk(const void* p, u32 bytes) { return __builtin_amdgcn_make_buffer_rsrc((void*)p, 0, bytes, 0x00020000); }
DEV float lo16(u32 v) { return __builtin_bit_cast(float, v << 16); }
DEV float hi16(u32 v) { return __builtin_bit_cast(float, v & 0xffff0000u); }
DEV u32 pk(float a, float b) { f2 t = {a, b}; return __builtin_bit_cast(u32, __builtin_convertvector(t, bf2)); }
DEV u32 pkh(float a, float b) { f2 t = {a, b}; return __builtin_bit_cast(u32, __builtin_convertvector(t, h2)); }
DEV float rbf(float a) { return lo16(pk(a, a)); }
DEV float fbits(u32 v) { return __builtin_bit_cast(float, v); }
DEV u32 ubits(float v) { return __builtin_bit_cast(u32, v); }
DEV float dot2(u32 a, u32 b, float c) {
  return __builtin_amdgcn_fdot2_f32_bf16(__builtin_bit_cast(bf2, a), __builtin_bit_cast(bf2, b), c, false);
}
template <int CTRL, int RM = 0xf> DEV float dppf(float v) {
  return fbits(__builtin_amdgcn_update_dpp(0u, ubits(v), CTRL, RM, 0xf, true));
}
template <int CTRL, int RM = 0xf> DEV u32 dppu(u32 v) { return __builtin_amdgcn_update_dpp(0u, v, CTRL, RM, 0xf, true); }
DEV float wave_sum(float v) {
  v += dppf<0xB1>(v);
  v += dppf<0x4E>(v);
  v += dppf<0x141>(v);
  v += dppf<0x140>(v);
  v += dppf<0x142, 0xa>(v);
  v += dppf<0x143, 0xc>(v);
  return fbits(__builtin_amdgcn_readlane(ubits(v), 63));
}
DEV u32 umax(u32 a, u32 b) { return a > b ? a : b; }
DEV u32 wave_umax(u32 v) {
  v = umax(v, dppu<0xB1>(v));
  v = umax(v, dppu<0x4E>(v));
  v = umax(v, dppu<0x141>(v));
  v = umax(v, dppu<0x140>(v));
  v = umax(v, dppu<0x142, 0xa>(v));
  v = umax(v, dppu<0x143, 0xc>(v));
  return __builtin_amdgcn_readlane(v, 63);
}

extern __shared__ __attribute__((aligned(16))) char smem[];
template <class T> DEV T lds_ld(u32 off) { return *(LAS T*)(size_t)(off); }
template <class T> DEV void lds_st(u32 off, T v) { *(LAS T*)(size_t)(off) = v; }
DEV v2u lds_tr(u32 off) {
  return __builtin_bit_cast(v2u, __builtin_amdgcn_ds_read_tr16_b64_v4i16((LAS s4v*)(size_t)(off)));
}
DEV v4u tr_frag(u32 a0, u32 a1) { v2u x = lds_tr(a0), y = lds_tr(a1); return v4u{x.x, x.y, y.x, y.y}; }
DEV f16v mfma32(v4u a, v4u b, f16v c) {
  return __builtin_amdgcn_mfma_f32_32x32x16_bf16(__builtin_bit_cast(bf8, a), __builtin_bit_cast(bf8, b), c, 0, 0, 0);
}
DEV f4 mfma16(v4u a, v4u b, f4 c) {
  return __builtin_amdgcn_mfma_f32_16x16x32_bf16(__builtin_bit_cast(bf8, a), __builtin_bit_cast(bf8, b), c, 0, 0, 0);
}
DEV f4 mfma16h(v4u a, v4u b, f4 c) {
  return __builtin_amdgcn_mfma_f32_16x16x32_f16(__builtin_bit_cast(h8, a), __builtin_bit_cast(h8, b), c, 0, 0, 0);
}

#define BAR() asm volatile("s_waitcnt lgkmcnt(0)\n\ts_barrier" ::: "memory")
#define VM0() asm volatile("s_waitcnt vmcnt(0)" ::: "memory")
#define LD128(dst, rs, vo, so, mods) \
  asm volatile("buffer_load_dwordx4 %0, %1, %2, %3 offen " mods : "=v"(dst) : "v"(vo), "s"(rs), "s"(so) : "memory")
#define LD64(dst, rs, vo, so, mods) \
  asm volatile("buffer_load_dwordx2 %0, %1, %2, %3 offen " mods : "=v"(dst) : "v"(vo), "s"(rs), "s"(so) : "memory")
#define LD16(dst, rs, vo, so, mods) \
  asm volatile("buffer_load_ushort %0, %1, %2, %3 offen " mods : "=v"(dst) : "v"(vo), "s"(rs), "s"(so) : "memory")
#define DMA(rs, vo, so, m, mods)                                                                          \
  asm volatile("s_mov_b32 m0, %3\n\ts_nop 0\n\tbuffer_load_dwordx4 %0, %1, %2 offen " mods " lds" ::"v"(vo), \
               "s"(rs), "s"(so), "s"(m)                                                                   \
               : "memory")
DEV void st128_dev(v4u v, rsrc_t rs, u32 vo, u32 so) {
  asm volatile("buffer_store_dwordx4 %0, %1, %2, %3 offen sc1\n\ts_nop 1" ::"v"(v), "v"(vo), "s"(rs), "s"(so) : "memory");
}
DEV void st64_dev(v2u v, rsrc_t rs, u32 vo, u32 so) { __builtin_amdgcn_raw_buffer_store_b64(v, rs, vo, so, 16); }
DEV bool tag_ok(v4u g, u32 ta, u32 tb) { return g.y == ta && g.w == tb; }

#define STAMP(i)

DEV f2 cmul(f2 a, f2 b) { return f2{a.x * b.x - a.y * b.y, a.x * b.y + a.y * b.x}; }
DEV f2 cexpv(float al, float be, float v) {
  const float e = __builtin_amdgcn_exp2f(al * v);
  const float r = __builtin_amdgcn_fractf(be * v);
  return f2{e * __builtin_amdgcn_cosf(r), e * __builtin_amdgcn_sinf(r)};
}
DEV u32 kidx(int lag) {
  const u32 bits = pk((float)lag, 0.f) & 0xffffu;
  const int id = lag < 256 ? lag : (int)bits - 0x4380 + 256;
  return (u32)(lag < 0 ? 6 : 8 + 2 * id);
}

DEV void stage_w(rsrc_t rg, rsrc_t ru, u32 vo, int unit, int slot, int ii, int gj) {
  const u32 so = unit * (32 * 3584) + gj * 224;
  const u32 m = L_STG + slot * SLOT + ii * 1024;
  DMA(rg, vo, so, m, "");
  DMA(ru, vo, so, m + UPOFF, "");
}
DEV u32 stage_vo(int ii, int l) { const u32 u = ii * 64 + l, row = u / 14; return row * 3584 + (u - row * 14) * 16; }

__global__ void __launch_bounds__(512, 2) fused_kernel(Params p) {
  const int tid = threadIdx.x, l = tid & 63;
  const int w = __builtin_amdgcn_readfirstlane(tid >> 6);
  const int b = blockIdx.x, xcd = b & 7, r = b >> 3;
  const int cp = xcd * 32 + r;
  const int gi = (xcd >> 1) * 4 + (r >> 3), gj = (xcd & 1) * 8 + (r & 7);
  const bool ismod = r < 24;
  const int mgrp = xcd * 6 + (r >> 2), mkq = r & 3;
  const rsrc_t rws = mk(p.ws, WS_END);
  STAMP(0);

  v2u nonce; v4u row, wm0, wm1; u32 pAr, pAi, pBr, pBi, pCr, pCi, c0, c1;
  {
    const u32 zero = 0;
    LD64(nonce, rws, zero, zero, "sc1");
    const rsrc_t rx = mk(p.x, kL * kD * 2);
    LD128(row, rx, l * 16, (b * 8 + w) * 1024, "nt");
    const u32 vo = (((w >> 1) & 1) * 64 + l) * 2, so = (2 * cp + (w >> 2)) * 256;
    LD16(pAr, mk(p.are, kD * kN * 2), vo, so, "nt");
    LD16(pAi, mk(p.aim, kD * kN * 2), vo, so, "nt");
    LD16(pBr, mk(p.bre, kD * kN * 2), vo, so, "nt");
    LD16(pBi, mk(p.bim, kD * kN * 2), vo, so, "nt");
    LD16(pCr, mk(p.cre, kD * kN * 2), vo, so, "nt");
    LD16(pCi, mk(p.cim, kD * kN * 2), vo, so, "nt");
    if (ismod) {
      const rsrc_t rc = mk(p.c, kD * 2), rm = mk(p.wmod, kD * 6 * kD * 2);
      LD16(c0, rc, (tid >> 3) * 2, mkq * 256, "");
      LD16(c1, rc, (tid >> 3) * 2, mkq * 256 + 128, "");
      const u32 vm = (tid >> 3) * 6144 + (tid & 7) * 16, sm = mkq * 128 * 6144 + mgrp * 128;
      LD128(wm0, rm, vm, sm, "nt");
      LD128(wm1, rm, vm, sm + 64 * 6144, "nt");
    }
  }
  const u32 ldt2 = ((const CAS u32*)p.logdt)[cp], dsk2 = ((const CAS u32*)p.dskip)[cp];
  u32 kix[2][7];
#pragma unroll
  for (int k = 0; k < 2; k++)
#pragma unroll
    for (int e = 0; e < 7; e++) kix[k][e] = L_G + (tid >> 8) * 1352 + kidx(4 * ((tid & 255) + 256 * k) + e);
  const u32 svo0 = stage_vo(w < 7 ? w : 0, l), svo1 = stage_vo(w < 3 ? w + 4 : 0, l);
  if (tid < 4) lds_st<u32>(M_CNT + tid * 4, 0u);
  if (tid < 2) lds_st<v2u>(L_G + tid * 1352, v2u{0u, 0u});

#pragma unroll
  for (int k = 0; k < 2; k++)
    asm volatile("" : "+v"(kix[k][0]), "+v"(kix[k][1]), "+v"(kix[k][2]), "+v"(kix[k][3]), "+v"(kix[k][4]), "+v"(kix[k][5]), "+v"(kix[k][6]));
  if (ismod) asm volatile("s_waitcnt vmcnt(4)" ::: "memory");
  else asm volatile("s_waitcnt vmcnt(0)" ::: "memory");
  asm volatile("" : "+v"(nonce), "+v"(row), "+v"(pAr), "+v"(pAi), "+v"(pBr), "+v"(pBi), "+v"(pCr), "+v"(pCi)::"memory");
  STAMP(1);
  const u64 tag64 = (((u64)(u32)__builtin_amdgcn_readfirstlane(nonce.y) << 32) | (u64)(u32)__builtin_amdgcn_readfirstlane(nonce.x)) + 0x9E3779B97F4A7C15ull;
  const u32 tagA = (u32)tag64, tagB = (u32)(tag64 >> 32);
  {
    lds_st<v4u>(L_ROW + w * 1040 + l * 16, row);
    float s = dot2(row.x, 0x3f803f80u, 0.f), q = dot2(row.x, row.x, 0.f);
    s = dot2(row.y, 0x3f803f80u, s); q = dot2(row.y, row.y, q);
    s = dot2(row.z, 0x3f803f80u, s); q = dot2(row.z, row.z, q);
    s = dot2(row.w, 0x3f803f80u, s); q = dot2(row.w, row.w, q);
    const float mean = wave_sum(s) * (1.f / kD);
    const float var = rbf(fmaxf(wave_sum(q) * (1.f / kD) - mean * mean, 0.f));
    const float den = __builtin_amdgcn_sqrtf(rbf(var + 1e-5f));
    if (l == 0) lds_st<u32>(M_RST + w * 4, pk(mean, den));
  }
  BAR();
  {
    const u32 a = L_ROW + (tid & 3) * 2080 + (tid >> 2) * 4;
    const u32 d0 = lds_ld<u32>(a), d1 = lds_ld<u32>(a + 1040), d2 = lds_ld<u32>(a + 512), d3 = lds_ld<u32>(a + 512 + 1040);
    st128_dev(v4u{d0, tagA, d1, tagB}, rws, tid * 16, WS_XG + b * 16384);
    st128_dev(v4u{d2, tagA, d3, tagB}, rws, tid * 16, WS_XG + b * 16384 + 8192);
  }
  if (w == 7 && l < 4) {
    const v2u s2 = lds_ld<v2u>(M_RST + l * 8);
    st128_dev(v4u{s2.x, tagA, s2.y, tagB}, rws, l * 16, WS_STG + b * 64);
  }
  const rsrc_t rwg = mk(p.wg, kD * kF * 2), rwu = mk(p.wu, kD * kF * 2);
  if (w < 7) {
#pragma unroll
    for (int u = 0; u < 5; u++) stage_w(rwg, rwu, svo0, u, u, w, gj);
  }
  STAMP(2);
  {
    const int ch = w >> 2, mode = ((w >> 1) & 1) * 64 + l;
    const float dt = rbf(expf(ch ? hi16(ldt2) : lo16(ldt2)));
    const float al = (dt * lo16(pAr)) * 1.44269504088896341f, be = (dt * lo16(pAi)) * 0.159154943091895336f;
    const u32 fsw = ((mode >> 1) & 1) << 2 | ((mode >> 2) & 1) << 1 | (mode & 1);
    const u32 base = L_FACT + ch * 32768 + (w & 1) * 16384 + mode * 128;
    if ((w & 1) == 0) {
      const f2 e8 = cexpv(al, be, 8.f);
      f2 v[4][8];
      v[0][0] = f2{1.f, 0.f};
      v[1][0] = cexpv(al, be, 64.f); v[2][0] = cexpv(al, be, 128.f); v[3][0] = cexpv(al, be, 192.f);
#pragma unroll
      for (int i = 1; i < 8; i++)
#pragma unroll
        for (int c = 0; c < 4; c++) v[c][i] = cmul(v[c][i - 1], e8);
#pragma unroll
      for (int c = 0; c < 4; c++) {
        lds_st<v4u>(base + ((c ^ fsw) << 4), v4u{pkh(v[c][0].x, v[c][1].x), pkh(v[c][2].x, v[c][3].x), pkh(v[c][4].x, v[c][5].x), pkh(v[c][6].x, v[c][7].x)});
        lds_st<v4u>(base + (((4 + c) ^ fsw) << 4), v4u{pkh(v[c][0].y, v[c][1].y), pkh(v[c][2].y, v[c][3].y), pkh(v[c][4].y, v[c][5].y), pkh(v[c][6].y, v[c][7].y)});
      }
    } else {
      const f2 e1 = cexpv(al, be, 1.f), e256 = cexpv(al, be, 256.f);
      f2 wv = cmul(f2{lo16(pCr), lo16(pCi)}, f2{dt * lo16(pBr), dt * lo16(pBi)});
      u32 ex = wave_umax(umax(ubits(wv.x) & 0x7fffffffu, ubits(wv.y) & 0x7fffffffu)) >> 23;
      ex = ex < 14 ? 14 : (ex > 254 ? 254 : ex);
      wv *= fbits((267 - ex) << 23);
      if (l == 0) lds_st<u32>(M_INV + (w >> 1) * 4, (ex - 13) << 23);
      const f2 e2 = cmul(e1, e1), e4 = cmul(e2, e2);
      f2 v[24];
      v[0] = wv;
#pragma unroll
      for (int i = 1; i < 8; i++) v[i] = cmul(v[i - 1], e1);
      v[8] = cmul(wv, e256);
      v[12] = cmul(v[8], e256); v[14] = cmul(v[12], e256); v[16] = cmul(v[14], e256);
      v[9] = cmul(v[8], e2); v[10] = cmul(v[8], e4); v[11] = cmul(v[10], e2);
      v[13] = cmul(v[12], e4); v[15] = cmul(v[14], e4);
#pragma unroll
      for (int i = 17; i < 21; i++) v[i] = cmul(v[i - 1], e256);
      v[21] = v[22] = v[23] = f2{0.f, 0.f};
#pragma unroll
      for (int c = 0; c < 3; c++) {
        const f2* q = v + 8 * c;
        lds_st<v4u>(base + ((c ^ fsw) << 4), v4u{pkh(q[0].x, q[1].x), pkh(q[2].x, q[3].x), pkh(q[4].x, q[5].x), pkh(q[6].x, q[7].x)});
        lds_st<v4u>(base + (((4 + c) ^ fsw) << 4), v4u{pkh(-q[0].y, -q[1].y), pkh(-q[2].y, -q[3].y), pkh(-q[4].y, -q[5].y), pkh(-q[6].y, -q[7].y)});
      }
    }
  }
  STAMP(3);
  if (ismod) {
    if (w < 7) asm volatile("s_waitcnt vmcnt(12)" ::: "memory");
    else asm volatile("s_waitcnt vmcnt(3)" ::: "memory");
    asm volatile("" : "+v"(c0), "+v"(c1), "+v"(wm0), "+v"(wm1)::"memory");
    const float ca = lo16(c0), cb = lo16(c1);
    const float sa = rbf(ca / (1.f + expf(-ca))), sb = rbf(cb / (1.f + expf(-cb)));
    float a[8];
#pragma unroll
    for (int e = 0; e < 4; e++) {
      a[2 * e] = sa * lo16(wm0[e]) + sb * lo16(wm1[e]);
      a[2 * e + 1] = sa * hi16(wm0[e]) + sb * hi16(wm1[e]);
    }
    float t[4], u[2];
#pragma unroll
    for (int e = 0; e < 4; e++) {
      auto s = __builtin_amdgcn_permlane32_swap(ubits(a[e]), ubits(a[e + 4]), false, false);
      t[e] = fbits(s[0]) + fbits(s[1]);
    }
#pragma unroll
    for (int e = 0; e < 2; e++) {
      auto s = __builtin_amdgcn_permlane16_swap(ubits(t[e]), ubits(t[e + 2]), false, false);
      u[e] = fbits(s[0]) + fbits(s[1]);
      u[e] += dppf<0x128>(u[e]);
    }
    if ((l & 8) == 0) lds_st<f2>(L_MODP + w * 256 + ((l & 7) * 8 + (l >> 5) * 4 + ((l >> 4) & 1) * 2) * 4, f2{u[0], u[1]});
  }
  BAR();
  if (ismod && w == 7 && l < 32) {
    f2 s = lds_ld<f2>(L_MODP + l * 8);
#pragma unroll
    for (int k = 1; k < 8; k++) s += lds_ld<f2>(L_MODP + k * 256 + l * 8);
    st128_dev(v4u{ubits(s.x), tagA, ubits(s.y), tagB}, rws, l * 16, WS_MODG + mkq * 24576 + mgrp * 512);
  }
  {
    const int ch = w >> 2, mt = (w >> 1) & 1, nt = w & 1, q = l >> 4, rq = (l >> 2) & 3, pp = l & 3;
    f4 acc[2] = {f4{0, 0, 0, 0}, f4{0, 0, 0, 0}};
    u32 aa[2], ab[2];
#pragma unroll
    for (int rd = 0; rd < 2; rd++) {
      const u32 m = 4 * q + 2 * rd + (rq >> 1);
      const u32 fsw = ((m >> 1) & 1) << 2 | ((m >> 2) & 1) << 1 | (m & 1);
      const u32 base = L_FACT + ch * 32768 + m * 128 + (pp & 1) * 8;
      aa[rd] = base + 16384 + ((((rq & 1) * 4 + mt * 2 + (pp >> 1)) ^ fsw) << 4);
      ab[rd] = base + ((((rq & 1) * 4 + nt * 2 + (pp >> 1)) ^ fsw) << 4);
    }
#pragma unroll
    for (int ks = 0; ks < 8; ks++)
      acc[ks >> 2] = mfma16h(tr_frag(aa[0] + ks * 2048, aa[1] + ks * 2048), tr_frag(ab[0] + ks * 2048, ab[1] + ks * 2048), acc[ks >> 2]);
    const f2 inv = lds_ld<f2>(M_INV + ch * 8);
    const f4 kv = acc[0] * inv.x + acc[1] * inv.y;
    const u32 p01 = pk(kv.x, kv.y), p23 = pk(kv.z, kv.w), bp = 16 * nt + (l & 15), g = L_G + ch * 1352 + 8;
    if (mt == 0) {
      if (q < 3) lds_st<v2u>(g + (q < 2 ? 8 * bp + 4 * q : 256 + 4 * bp) * 2, v2u{p01, p23});
      else { lds_st<u32>(g + (384 + 2 * bp) * 2, p01); lds_st<u32>(g + (448 + 2 * bp) * 2, p23); }
    } else if (q == 0) {
      lds_st<u16>(g + (512 + bp) * 2, (u16)p01); lds_st<u16>(g + (544 + bp) * 2, (u16)(p01 >> 16));
      lds_st<u16>(g + (576 + bp) * 2, (u16)p23); lds_st<u16>(g + (608 + bp) * 2, (u16)(p23 >> 16));
    } else if (q == 1) lds_st<u16>(g + (640 + bp) * 2, (u16)p01);
  }
  BAR();
  STAMP(4);
  v4u gx0, gx1, gs0, gs1, gm;
  const u32 vgx = (tid >> 1) * 16384 + (tid & 1) * 32, lm = l < 12 ? l : 11;
  const u32 vgm = (lm & 3) * 24576 + (lm >> 2) * 4096;
  {
#pragma unroll
    for (int k = 0; k < 2; k++) {
      u32 v[7];
#pragma unroll
      for (int e = 0; e < 7; e++) v[e] = lds_ld<u16>(kix[k][e]);
      const u32 a = L_KC + (tid >> 8) * 16640 + ((tid & 255) + 256 * k + 4) * 8;
#pragma unroll
      for (int s = 0; s < 4; s++) lds_st<v2u>(a + s * 4160, v2u{v[s] | v[s + 1] << 16, v[s + 2] | v[s + 3] << 16});
    }
    if (tid < 8) {
      u32 v[7];
#pragma unroll
      for (int e = 0; e < 7; e++) v[e] = lds_ld<u16>(L_G + (tid >> 2) * 1352 + kidx(4 * (tid & 3) - 16 + e));
      const u32 a = L_KC + (tid >> 2) * 16640 + (tid & 3) * 8;
#pragma unroll
      for (int s = 0; s < 4; s++) lds_st<v2u>(a + s * 4160, v2u{v[s] | v[s + 1] << 16, v[s + 2] | v[s + 3] << 16});
    }
    if (tid < 64) lds_st<v4u>(L_HR + (tid >> 5) * 4608 + 4096 + (tid & 31) * 16, v4u{0u, 0u, 0u, 0u});
  }
  STAMP(5);
  for (u32 spin = 0;; spin++) {
    LD128(gx0, rws, vgx, WS_XG + cp * 64, "sc1");
    LD128(gx1, rws, vgx, WS_XG + cp * 64 + 16, "sc1");
    LD128(gs0, rws, tid * 32, WS_STG, "sc1");
    LD128(gs1, rws, tid * 32, WS_STG + 16, "sc1");
    LD128(gm, rws, vgm, WS_MODG + cp * 16, "sc1");
    asm volatile("s_waitcnt vmcnt(0)" : "+v"(gx0), "+v"(gx1), "+v"(gs0), "+v"(gs1), "+v"(gm)::"memory");
    const bool ok = tag_ok(gx0, tagA, tagB) && tag_ok(gx1, tagA, tagB) && tag_ok(gs0, tagA, tagB) && tag_ok(gs1, tagA, tagB) && tag_ok(gm, tagA, tagB);
    if (__all(ok) || spin > kSpin) break;
    __builtin_amdgcn_s_sleep(4);
  }
  STAMP(6);
  float ga0, ga1;
  {
    float m0 = fbits(gm.x), m1 = fbits(gm.z);
    m0 += dppf<0xB1>(m0); m1 += dppf<0xB1>(m1);
    m0 += dppf<0x4E>(m0); m1 += dppf<0x4E>(m1);
    const u32 mm = pk(m0, m1);
    const u32 msa = __builtin_amdgcn_readlane(mm, 0), mca = __builtin_amdgcn_readlane(mm, 4), mga = __builtin_amdgcn_readlane(mm, 8);
    const float sa0 = lo16(msa), sa1 = hi16(msa), s0 = rbf(1.f + lo16(mca)), s1 = rbf(1.f + hi16(mca));
    ga0 = lo16(mga); ga1 = hi16(mga);
    const u32 xs[4] = {gx0.x, gx0.z, gx1.x, gx1.z}, st[4] = {gs0.x, gs0.z, gs1.x, gs1.z};
    float h0[4], h1[4];
#pragma unroll
    for (int e = 0; e < 4; e++) {
      const float m = lo16(st[e]), rd = __builtin_amdgcn_rcpf(hi16(st[e]));
      const u32 xn = pk((lo16(xs[e]) - m) * rd, (hi16(xs[e]) - m) * rd);
      h0[e] = lo16(xn) * s0 + sa0; h1[e] = hi16(xn) * s1 + sa1;
    }
    lds_st<v2u>(L_HR + (2044 - 4 * tid) * 2, v2u{pk(h0[3], h0[2]), pk(h0[1], h0[0])});
    lds_st<v2u>(L_HR + 4608 + (2044 - 4 * tid) * 2, v2u{pk(h1[3], h1[2]), pk(h1[1], h1[0])});
    lds_st<v2u>(L_XB + tid * 8, v2u{(xs[0] & 0xffffu) | xs[1] << 16, (xs[2] & 0xffffu) | xs[3] << 16});
    lds_st<v2u>(L_XB + 4096 + tid * 8, v2u{xs[0] >> 16 | (xs[1] & 0xffff0000u), xs[2] >> 16 | (xs[3] & 0xffff0000u)});
  }
  BAR();
  STAMP(7);
  if (b == 0 && tid == 0) st64_dev(v2u{tagA, tagB}, rws, 0, WS_NONCE);

  v4u W[32];
  const u32 wtr = ((l >> 5) * 8 + ((l >> 2) & 3)) * 224 + ((l >> 1) & 1) * UPOFF + (w * 16 + ((l >> 4) & 1) * 8 + (l & 1) * 4) * 2 + L_STG;
#define READ_W(u0, n)                                                                            \
  _Pragma("unroll") for (int u = 0; u < (n); u++) _Pragma("unroll") for (int k = 0; k < 2; k++)  \
      W[2 * ((u0) + u) + k] = tr_frag(wtr + u * SLOT + k * 3584, wtr + u * SLOT + k * 3584 + 896)
  if (w < 7) { READ_W(0, 5); }
  const int t4 = tid & 255;
  const int ech = t4 >> 7;
  const float gav = ech ? ga1 : ga0, dsk = ech ? hi16(dsk2) : lo16(dsk2);
  auto conv_epilogue = [&](int half) {
    const int tile = (t4 >> 5) & 3, n = (t4 >> 1) & 15, qp = t4 & 1, t0 = 1024 * half + 256 * tile + 16 * n + 8 * qp;
    const u32 pa = L_P + ech * 4096 + tile * 1024 + (n + 32 * qp) * 16;
    const f4 y0 = lds_ld<f4>(pa) + lds_ld<f4>(pa + 8192), y1 = lds_ld<f4>(pa + 256) + lds_ld<f4>(pa + 8192 + 256);
    const v4u hv = lds_ld<v4u>(L_HR + ech * 4608 + (2040 - t0) * 2), xv = lds_ld<v4u>(L_XB + ech * 4096 + t0 * 2);
    const float y[8] = {y0.x, y0.y, y0.z, y0.w, y1.x, y1.y, y1.z, y1.w};
    v4u o;
#pragma unroll
    for (int e = 0; e < 4; e++) {
      const u32 yr = pk(y[2 * e], y[2 * e + 1]), hd = pk(hi16(hv[3 - e]) * dsk, lo16(hv[3 - e]) * dsk);
      const u32 y2 = pk(lo16(yr) + lo16(hd), hi16(yr) + hi16(hd));
      const u32 z = pk(gav * lo16(y2), gav * hi16(y2));
      o[e] = pk(lo16(xv[e]) + lo16(z), hi16(xv[e]) + hi16(z));
    }
    st128_dev(o, rws, (t0 >> 6) * 65536 + (t0 & 63) * 2 + ech * 128, WS_X1T + cp * 256);
  };
  auto publish_x1 = [&](int half) {
    u32 old = 0;
    if (l == 0) old = __hip_atomic_fetch_add((LAS u32*)(size_t)(M_CNT + half * 4), 1u, __ATOMIC_RELAXED, __HIP_MEMORY_SCOPE_WORKGROUP);
    if (__builtin_amdgcn_readfirstlane(old) == 3 && l < 8) st64_dev(v2u{tagA, tagB}, rws, l * 8192, WS_F2 + half * 65536 + b * 32);
  };
  if (w < 4) {
    const int ch = w & 1, bh = w >> 1, m = l & 15, q = l >> 4;
    const int lag0 = 16 * (q >> 1) + m - 8 * (q & 1) - 7, sg = (m + 1) & 3;
    const u32 ka = L_KC + ch * 16640 + sg * 4160 + (lag0 - sg + 16) * 2 + bh * 256;
    const u32 ha = L_HR + ch * 4608 + 4080 - 3584 - 32 * m + 32 * (q >> 1) - 16 * (q & 1) + bh * 256;
    auto ldA = [&](int a, int bb) { const v2u x = lds_ld<v2u>(ka + 512 * a + 64 * bb), y = lds_ld<v2u>(ka + 512 * a + 64 * bb + 8); return v4u{x.x, x.y, y.x, y.y}; };
    auto ldB = [&](int i, int bb) { return lds_ld<v4u>(ha + 512 * (7 - i) + 64 * bb); };
    f4 acc[4];
#pragma unroll
    for (int i = 0; i < 4; i++) acc[i] = f4{0, 0, 0, 0};
#pragma unroll
    for (int bb = 0; bb < 4; bb++) {
      v4u A[4], B[4];
#pragma unroll
      for (int i = 0; i < 4; i++) { A[i] = ldA(i, bb); B[i] = ldB(i, bb); }
#pragma unroll
      for (int a = 0; a < 4; a++)
#pragma unroll
        for (int i = a; i < 4; i++) acc[i] = mfma16(A[a], B[i - a], acc[i]);
    }
#pragma unroll
    for (int i = 0; i < 4; i++) lds_st<f4>(L_P + w * 4096 + i * 1024 + l * 16, acc[i]);
    BAR();
    STAMP(8);
#pragma unroll
    for (int u = 0; u < 6; u++) {
      stage_w(rwg, rwu, svo0, 5 + u, u, w, gj);
      if (w < 3) stage_w(rwg, rwu, svo1, 5 + u, u, w + 4, gj);
    }
#pragma unroll
    for (int i = 0; i < 4; i++) acc[i] = f4{0, 0, 0, 0};
#pragma unroll
    for (int bb = 0; bb < 4; bb++) {
      v4u B[8];
#pragma unroll
      for (int i = 0; i < 8; i++) B[i] = ldB(i, bb);
#pragma unroll
      for (int a = 0; a < 8; a++) {
        const v4u A = ldA(a, bb);
#pragma unroll
        for (int i = (a > 4 ? a : 4); i < 8; i++) acc[i - 4] = mfma16(A, B[i - a], acc[i - 4]);
      }
    }
    STAMP(9);
    BAR();
#pragma unroll
    for (int i = 0; i < 4; i++) lds_st<f4>(L_P + w * 4096 + i * 1024 + l * 16, acc[i]);
    VM0();
    BAR();
  } else {
    BAR();
    STAMP(8);
    conv_epilogue(0);
    VM0();
    publish_x1(0);
    v4u g[9];
    const u32 tg = t4 < 128 ? t4 : 127;
    for (u32 spin = 0;; spin++) {
#pragma unroll
      for (int k = 0; k < 4; k++) {
        LD128(g[k], rws, t4 * 16, WS_MODG + k * 24576 + 768 * 16, "sc1");
        LD128(g[4 + k], rws, t4 * 16, WS_MODG + k * 24576 + 1024 * 16, "sc1");
      }
      LD128(g[8], rws, (tg & 3) * 24576 + (tg >> 2) * 16, WS_MODG + (1280 + (gj & 7) * 32) * 16, "sc1");
      asm volatile("s_waitcnt vmcnt(0)" : "+v"(g[0]), "+v"(g[1]), "+v"(g[2]), "+v"(g[3]), "+v"(g[4]), "+v"(g[5]), "+v"(g[6]), "+v"(g[7]), "+v"(g[8])::"memory");
      bool ok = true;
#pragma unroll
      for (int k = 0; k < 9; k++) ok = ok && tag_ok(g[k], tagA, tagB);
      if (__all(ok) || spin > kSpin) break;
      __builtin_amdgcn_s_sleep(4);
    }
    const u32 sbp = pk((fbits(g[0].x) + fbits(g[1].x)) + (fbits(g[2].x) + fbits(g[3].x)), (fbits(g[0].z) + fbits(g[1].z)) + (fbits(g[2].z) + fbits(g[3].z)));
    const u32 cbp = pk((fbits(g[4].x) + fbits(g[5].x)) + (fbits(g[6].x) + fbits(g[7].x)), (fbits(g[4].z) + fbits(g[5].z)) + (fbits(g[6].z) + fbits(g[7].z)));
    lds_st<u32>(L_V + t4 * 4, pk(1.f + lo16(cbp), 1.f + hi16(cbp)));
    lds_st<u32>(L_V + 1024 + t4 * 4, sbp);
    float m0 = fbits(g[8].x), m1 = fbits(g[8].z);
    m0 += dppf<0xB1>(m0); m1 += dppf<0xB1>(m1);
    m0 += dppf<0x4E>(m0); m1 += dppf<0x4E>(m1);
    if (t4 < 128 && (t4 & 3) == 0) lds_st<f2>(L_GB + (t4 >> 2) * 8, f2{rbf(m0), rbf(m1)});
    STAMP(9);
    BAR();
    BAR();
    conv_epilogue(1);
  }
  STAMP(10);
  if (w < 7) { READ_W(5, 6); }
  BAR();
  if (w < 4) {
#pragma unroll
    for (int u = 0; u < 5; u++) {
      stage_w(rwg, rwu, svo0, 11 + u, u, w, gj);
      if (w < 3) stage_w(rwg, rwu, svo1, 11 + u, u, w + 4, gj);
    }
  }
  {
    const u32 sv = lds_ld<u16>(L_V + tid * 2), e = tid & 7, sh = sv << ((e & 1) * 16);
    lds_st<v4u>(L_DIAG + (tid >> 4) * 336 + (tid & 15) * 16, v4u{(e >> 1) == 0 ? sh : 0u, (e >> 1) == 1 ? sh : 0u, (e >> 1) == 2 ? sh : 0u, (e >> 1) == 3 ? sh : 0u});
    if (tid < 128) lds_st<v4u>(L_DIAG + (tid >> 2) * 336 + 256 + (tid & 3) * 16, lds_ld<v4u>(L_V + ((tid >> 1) & 1) * 1024 + (tid >> 2) * 32 + (tid & 1) * 16));
    if (tid < 32) lds_st<v4u>(L_DIAG + tid * 336 + 320, v4u{0u, 0u, 0u, 0u});
  }
  auto wait_x1 = [&](int half) {
    for (u32 spin = 0;; spin++) {
      v2u f0, f1, f2_, f3;
      const u32 so = WS_F2 + half * 65536 + xcd * 8192;
      LD64(f0, rws, l * 32, so, "sc1");
      LD64(f1, rws, l * 32, so + 2048, "sc1");
      LD64(f2_, rws, l * 32, so + 4096, "sc1");
      LD64(f3, rws, l * 32, so + 6144, "sc1");
      asm volatile("s_waitcnt vmcnt(0)" : "+v"(f0), "+v"(f1), "+v"(f2_), "+v"(f3)::"memory");
      const bool ok = f0.x == tagA && f0.y == tagB && f1.x == tagA && f1.y == tagB && f2_.x == tagA && f2_.y == tagB && f3.x == tagA && f3.y == tagB;
      if (__all(ok) || spin > kSpin) break;
      __builtin_amdgcn_s_sleep(4);
    }
  };
  if (w >= 4) { VM0(); publish_x1(1); }
  BAR();
  STAMP(11);
  f2 stv = {0.f, 0.f};
  const u32 dga = L_DIAG + ((l & 31) < 16 ? (((l >> 3) & 1) == (l >> 5) ? (l & 15) * 16 : 320) : ((l & 31) < 18 ? 256 + (l & 1) * 32 + (l >> 5) * 16 : 320));
#define PREP_W(s0, s1)                                                                       \
  _Pragma("unroll") for (int s = (s0); s < (s1); s++) {                                      \
    const f16v d = mfma32(lds_ld<v4u>(dga + s * 336), W[s], f16v{});                         \
    W[s] = v4u{pk(d[0], d[1]), pk(d[2], d[3]), pk(d[4], d[5]), pk(d[6], d[7])};              \
    stv += f2{d[8], d[9]};                                                                   \
  }
  const u32 tvo = (l >> 3) * 128 + (((l & 7) ^ (((l >> 4) & 1) << 2)) << 4);
  if (w < 7) { PREP_W(0, 22); VM0(); }
  else wait_x1(0);
  STAMP(12);
  BAR();
#pragma unroll
  for (int k = 0; k < 8; k++) DMA(rws, tvo, WS_X1T + gi * 65536 + (w * 8 + k) * 1024, L_TA + (w * 8 + k) * 1024, "sc1");
  if (w < 7) {
    READ_W(11, 5);
    PREP_W(22, 32);
    if (l < 32) {
      lds_st<float>(L_ST + w * 256 + l * 4, stv.x);
      lds_st<float>(L_ST + w * 256 + 128 + l * 4, (l & 8) ? stv.y : stv.y * -1.44269504088896341f);
    }
  }
  STAMP(13);
  VM0();
  BAR();
  STAMP(14);

  const u32 tra = ((l >> 5) * 4 + ((l >> 2) & 3)) * 128 + ((((l >> 4) & 1) * 32 + (l & 3) * 8) ^ (((l >> 3) & 1) << 6));
  const u32 rra = ((gj & 7) * 64 + (w >> 1) * 16 + (l >> 4) * 4 + ((l >> 2) & 3)) * 128 + (((gj >> 3) * 64 + (w & 1) * 32 + (l & 3) * 8) ^ (((l >> 3) & 1) << 6));
  const rsrc_t rwd = mk(p.wd, kF * kD * 2);
  const u32 dvo = (l >> 3) * 1024 + (((l & 7) ^ (((l >> 4) & 1) << 2)) << 4);
  const u32 d1base = w < 6 ? L_X + w * 10240 : 138752 + (w - 6) * 10240;
  f16v acc[2];
  auto gemm1 = [&](u32 tb, int s0, int s1) {
#pragma unroll
    for (int s = 0; s < 32; s++)
      if (s >= s0 && s < s1) {
        acc[0] = mfma32(W[s], tr_frag(tb + tra + s * 2048, tb + tra + s * 2048 + 1024), acc[0]);
        acc[1] = mfma32(W[s], tr_frag(tb + (tra ^ 64) + s * 2048, tb + (tra ^ 64) + s * 2048 + 1024), acc[1]);
      }
  };
  auto ln2_stats = [&](u32 tb, int half) {
#pragma unroll
    for (int nt = 0; nt < 2; nt++) {
      float s = 0.f, q = 0.f, s2 = 0.f, q2 = 0.f;
      const u32 a = tb + (tra ^ (nt * 64));
#pragma unroll 8
      for (int k = 0; k < 32; k++) {
        const v2u x = lds_tr(a + k * 2048), y = lds_tr(a + k * 2048 + 1024);
        s = dot2(x.x, 0x3f803f80u, s); q = dot2(x.x, x.x, q); s2 = dot2(x.y, 0x3f803f80u, s2); q2 = dot2(x.y, x.y, q2);
        s = dot2(y.x, 0x3f803f80u, s); q = dot2(y.x, y.x, q); s2 = dot2(y.y, 0x3f803f80u, s2); q2 = dot2(y.y, y.y, q2);
      }
      auto sw = __builtin_amdgcn_permlane32_swap(ubits(s + s2), ubits(q + q2), false, false);
      const float t = fbits(sw[0]) + fbits(sw[1]);
      auto s3 = __builtin_amdgcn_permlane32_swap(ubits(t), ubits(t), false, false);
      const float mean = fbits(s3[0]) * (1.f / kD);
      const float var = rbf(fmaxf(fbits(s3[1]) * (1.f / kD) - mean * mean, 0.f));
      const float den = rbf(__builtin_amdgcn_sqrtf(rbf(var + 1e-5f)));
      if (l < 32) lds_st<f2>(L_STAT + half * 512 + (nt * 32 + l) * 8, f2{rbf(mean), __builtin_amdgcn_rcpf(den)});
    }
  };
  auto glu_epilogue = [&](int half) {
    f4 S[4], T[4];
#pragma unroll
    for (int k = 0; k < 4; k++) {
      S[k] = lds_ld<f4>(L_ST + w * 256 + k * 32 + (l >> 5) * 16);
      T[k] = lds_ld<f4>(L_ST + w * 256 + 128 + k * 32 + (l >> 5) * 16);
    }
#pragma unroll
    for (int nt = 0; nt < 2; nt++) {
      const f2 st = lds_ld<f2>(L_STAT + half * 512 + (nt * 32 + (l & 31)) * 8);
      const float rg = st.y * -1.44269504088896341f;
      float o[8];
#pragma unroll
      for (int k = 0; k < 2; k++)
#pragma unroll
        for (int e = 0; e < 4; e++) {
          const float z = (acc[nt][8 * k + e] - st.x * S[2 * k][e]) * rg + T[2 * k][e];
          const float u = (acc[nt][8 * k + 4 + e] - st.x * S[2 * k + 1][e]) * st.y + T[2 * k + 1][e];
          o[4 * k + e] = u * __builtin_amdgcn_rcpf(1.f + __builtin_amdgcn_exp2f(z));
        }
      st128_dev(v4u{pk(o[0], o[1]), pk(o[2], o[3]), pk(o[4], o[5]), pk(o[6], o[7])}, rws, l * 16,
                WS_HH + ((half * 32 + 2 * gi + nt) * 112 + 7 * gj + w) * 1024);
    }
  };
  auto publish_hh = [&](int half) {
    u32 old = 0;
    if (l == 0) old = __hip_atomic_fetch_add((LAS u32*)(size_t)(M_CNT + 8 + half * 4), 1u, __ATOMIC_RELAXED, __HIP_MEMORY_SCOPE_WORKGROUP);
    if (__builtin_amdgcn_readfirstlane(old) == 6 && l == 0) st64_dev(v2u{tagA, tagB}, rws, 0, WS_F3 + ((half * 16 + gi) * 16 + gj) * 128);
  };
  auto stage_d = [&](int s0, int n, u32 base) {
#pragma unroll
    for (int k = 0; k < 2 * n; k++) DMA(rwd, dvo, (w * 224 + s0 * 16 + k * 8) * 1024 + (gj & 7) * 128, base + k * 1024, "");
  };
  const v2u resa = lds_tr(L_TA + rra);
  if (w < 7) {
    acc[0] = acc[1] = f16v{};
    gemm1(L_TA, 0, 32);
  } else {
    wait_x1(1);
#pragma unroll 8
    for (int k = 0; k < 64; k++) DMA(rws, tvo, WS_X1T + (16 + gi) * 65536 + k * 1024, L_TB + k * 1024, "sc1");
    ln2_stats(L_TA, 0);
    VM0();
  }
  STAMP(15);
  BAR();
  const v2u resb = lds_tr(L_TB + rra);
  if (w < 7) {
    glu_epilogue(0);
    acc[0] = acc[1] = f16v{};
    gemm1(L_TB, 0, 8);
    VM0();
    publish_hh(0);
    stage_d(0, 5, d1base);
    gemm1(L_TB, 8, 32);
  } else {
    stage_d(0, 5, d1base);
    ln2_stats(L_TB, 1);
  }
  STAMP(16);
  BAR();
  if (w < 7) glu_epilogue(1);
  VM0();
  if (w < 7) publish_hh(1);
  STAMP(17);

  v4u Wd[14][2];
#pragma unroll
  for (int s = 0; s < 5; s++)
#pragma unroll
    for (int mt = 0; mt < 2; mt++) Wd[s][mt] = tr_frag(d1base + (tra ^ (mt * 64)) + s * 2048, d1base + (tra ^ (mt * 64)) + s * 2048 + 1024);
  BAR();
  const u32 d2base = L_X + w * 18432;
  stage_d(5, 9, d2base);
  v4u hf[14];
  auto load_hh = [&](int half) {
    for (u32 spin = 0;; spin++) {
      v2u f;
      LD64(f, rws, (l & 1) * 128, WS_F3 + ((half * 16 + gi) * 16 + 2 * w) * 128, "sc1");
      asm volatile("s_waitcnt vmcnt(0)" : "+v"(f)::"memory");
      if (__all(f.x == tagA && f.y == tagB) || spin > kSpin) break;
      __builtin_amdgcn_s_sleep(4);
    }
#pragma unroll
    for (int s = 0; s < 14; s++)
      hf[s] = __builtin_amdgcn_raw_buffer_load_b128(rws, l * 16 + (s & 3) * 1024, WS_HH + ((half * 32 + 2 * gi + (gj >> 3)) * 112 + 14 * w + (s & ~3)) * 1024, 16);
  };
  auto gemm2 = [&](u32 red) {
    acc[0] = acc[1] = f16v{};
#pragma unroll
    for (int s = 0; s < 14; s++) {
      acc[0] = mfma32(Wd[s][0], hf[s], acc[0]);
      acc[1] = mfma32(Wd[s][1], hf[s], acc[1]);
    }
#pragma unroll
    for (int mt = 0; mt < 2; mt++)
#pragma unroll
      for (int g = 0; g < 4; g++)
        lds_st<f4>(red + w * 8192 + (mt * 4 + g) * 1024 + l * 16, f4{acc[mt][4 * g], acc[mt][4 * g + 1], acc[mt][4 * g + 2], acc[mt][4 * g + 3]});
  };
  const rsrc_t rout = mk(p.out, kL * kD * 2);
  auto out_epilogue = [&](int half, u32 red, v2u res) {
    const u32 a = red + ((w >> 1) * 4 + (l >> 4)) * 512 + ((w & 1) * 16 + (l & 15)) * 16;
    f4 s = lds_ld<f4>(a);
#pragma unroll
    for (int k = 1; k < 8; k++) s += lds_ld<f4>(a + k * 8192);
    const f4 gb = lds_ld<f4>(L_GB + ((w >> 1) * 4 + (l >> 4)) * 16);
    const u32 r01 = pk(s.x, s.y), r23 = pk(s.z, s.w);
    const u32 z01 = pk(gb.x * lo16(r01), gb.y * hi16(r01)), z23 = pk(gb.z * lo16(r23), gb.w * hi16(r23));
    const v2u o = {pk(lo16(res.x) + lo16(z01), hi16(res.x) + hi16(z01)), pk(lo16(res.y) + lo16(z23), hi16(res.y) + hi16(z23))};
    __builtin_amdgcn_raw_buffer_store_b64(o, rout, ((w & 1) * 16 + (l & 15)) * 1024 + ((w >> 1) * 4 + (l >> 4)) * 8,
                                          (half * 1024 + gi * 64 + (gj >> 3) * 32) * 1024 + (gj & 7) * 128, 0);
  };
  load_hh(0);
  STAMP(18);
#pragma unroll
  for (int s = 5; s < 14; s++)
#pragma unroll
    for (int mt = 0; mt < 2; mt++) Wd[s][mt] = tr_frag(d2base + (tra ^ (mt * 64)) + (s - 5) * 2048, d2base + (tra ^ (mt * 64)) + (s - 5) * 2048 + 1024);
  BAR();
  gemm2(L_X);
  STAMP(19);
  load_hh(1);
  STAMP(20);
  BAR();
  out_epilogue(0, L_X, resa);
  gemm2(L_STG);
  STAMP(21);
  BAR();
  out_epilogue(1, L_STG, resb);
  STAMP(22);
}

}

extern "C" void kernel_launch(void* const* d_in, const int* in_sizes, int n_in, void* d_out, int out_size, void* d_ws,
                              size_t ws_size, hipStream_t stream) {
  using namespace s4glu;
  (void)in_sizes; (void)n_in; (void)out_size; (void)ws_size;
  Params p;
  p.x = d_in[0]; p.are = d_in[1]; p.aim = d_in[2]; p.bre = d_in[3]; p.bim = d_in[4]; p.cre = d_in[5]; p.cim = d_in[6];
  p.logdt = d_in[7]; p.dskip = d_in[8]; p.wg = d_in[10]; p.wu = d_in[11]; p.wd = d_in[12]; p.c = d_in[13]; p.wmod = d_in[14];
  p.out = d_out; p.ws = d_ws;
  (void)hipFuncSetAttribute((const void*)fused_kernel, hipFuncAttributeMaxDynamicSharedMemorySize, L_END);
  fused_kernel<<<256, 512, L_END, stream>>>(p);
}
```

```cpp
#define AMP 1
#include <hip/hip_runtime.h>
#include <stdint.h>

#define DBG 0

namespace s4glu {

typedef unsigned short u16;
typedef unsigned u32;
typedef unsigned long long u64;
typedef u32 v4u __attribute__((ext_vector_type(4)));
typedef u32 v2u __attribute__((ext_vector_type(2)));
typedef float f2 __attribute__((ext_vector_type(2)));
typedef float f4 __attribute__((ext_vector_type(4)));
typedef float f16v __attribute__((ext_vector_type(16)));
typedef __bf16 bf2 __attribute__((ext_vector_type(2)));
typedef __bf16 bf8 __attribute__((ext_vector_type(8)));
typedef _Float16 h2 __attribute__((ext_vector_type(2)));
typedef _Float16 h8 __attribute__((ext_vector_type(8)));
typedef short s4v __attribute__((ext_vector_type(4)));
typedef __amdgpu_buffer_rsrc_t rsrc_t;
#define LAS __attribute__((address_space(3)))
#define CAS __attribute__((address_space(4)))
#define DEV __device__ __forceinline__

constexpr int kL = 2048, kD = 512, kN = 128, kF = 1792;

constexpr u32 WS_NONCE = 0;
constexpr u32 WS_STG = 4096;
constexpr u32 WS_MODG = WS_STG + 16384;
constexpr u32 WS_F2 = WS_MODG + 98304;
constexpr u32 WS_F3 = WS_F2 + 131072;
constexpr u32 WS_XG = 0x80000;
constexpr u32 WS_X1T = WS_XG + 4194304;
constexpr u32 WS_HH = WS_X1T + 2097152;
constexpr u32 WS_END = WS_HH + 7340032;
constexpr u32 WS_DBG = WS_END;
static_assert(WS_F3 + 65536 <= WS_XG, "ws map");

constexpr u32 L_V = 0;
constexpr u32 L_ST = 2048;
constexpr u32 L_STAT = 3840;
constexpr u32 L_GB = 4864;
constexpr u32 L_MISC = 5120;
constexpr u32 L_STAMP = 5376;
constexpr u32 L_X = 6144;
constexpr u32 L_FACT = L_X;
constexpr u32 L_KC = L_X;
constexpr u32 L_HR = L_KC + 33280;
constexpr u32 L_XB = L_HR + 9216;
constexpr u32 L_P = L_XB + 8192;
constexpr u32 L_TA = L_X;
constexpr u32 L_STG = L_X + 67072;
constexpr u32 SLOT = 14464, UPOFF = 7296;
constexpr u32 L_S5 = L_STG + 5 * SLOT;
constexpr u32 L_ROW = L_S5;
constexpr u32 L_MODP = L_ROW + 8320;
constexpr u32 L_G = L_MODP + 2048;
constexpr u32 L_DIAG = L_S5;
constexpr u32 L_TB = L_STG;
constexpr u32 L_END = 163840;
static_assert(L_G + 2704 <= L_STG + 6 * SLOT && L_STG + 6 * SLOT <= L_END, "lds map");
constexpr u32 M_CNT = L_MISC;
constexpr u32 M_RST = L_MISC + 32;
constexpr u32 M_INV = L_MISC + 64;

constexpr u32 kSpin = 40000;

struct Params {
  const void *x, *are, *aim, *bre, *bim, *cre, *cim, *logdt, *dskip, *wg, *wu, *wd, *c, *wmod;
  void *out, *ws;
};

DEV rsrc_t mk(const void* p, u32 bytes) { return __builtin_amdgcn_make_buffer_rsrc((void*)p, 0, bytes, 0x00020000); }
DEV float lo16(u32 v) { return __builtin_bit_cast(float, v << 16); }
DEV float hi16(u32 v) { return __builtin_bit_cast(float, v & 0xffff0000u); }
DEV u32 pk(float a, float b) { f2 t = {a, b}; return __builtin_bit_cast(u32, __builtin_convertvector(t, bf2)); }
DEV u32 pkh(float a, float b) { f2 t = {a, b}; return __builtin_bit_cast(u32, __builtin_convertvector(t, h2)); }
DEV float rbf(float a) { return lo16(pk(a, a)); }
DEV float fbits(u32 v) { return __builtin_bit_cast(float, v); }
DEV u32 ubits(float v) { return __builtin_bit_cast(u32, v); }
DEV float dot2(u32 a, u32 b, float c) {
  return __builtin_amdgcn_fdot2_f32_bf16(__builtin_bit_cast(bf2, a), __builtin_bit_cast(bf2, b), c, false);
}
template <int CTRL, int RM = 0xf> DEV float dppf(float v) {
  return fbits(__builtin_amdgcn_update_dpp(0u, ubits(v), CTRL, RM, 0xf, true));
}
template <int CTRL, int RM = 0xf> DEV u32 dppu(u32 v) { return __builtin_amdgcn_update_dpp(0u, v, CTRL, RM, 0xf, true); }
DEV float wave_sum(float v) {
  v += dppf<0xB1>(v);
  v += dppf<0x4E>(v);
  v += dppf<0x141>(v);
  v += dppf<0x140>(v);
  v += dppf<0x142, 0xa>(v);
  v += dppf<0x143, 0xc>(v);
  return fbits(__builtin_amdgcn_readlane(ubits(v), 63));
}
DEV u32 umax(u32 a, u32 b) { return a > b ? a : b; }
DEV u32 wave_umax(u32 v) {
  v = umax(v, dppu<0xB1>(v));
  v = umax(v, dppu<0x4E>(v));
  v = umax(v, dppu<0x141>(v));
  v = umax(v, dppu<0x140>(v));
  v = umax(v, dppu<0x142, 0xa>(v));
  v = umax(v, dppu<0x143, 0xc>(v));
  return __builtin_amdgcn_readlane(v, 63);
}

extern __shared__ __attribute__((aligned(16))) char smem[];
template <class T> DEV T lds_ld(u32 off) { return *(LAS T*)(size_t)(off); }
template <class T> DEV void lds_st(u32 off, T v) { *(LAS T*)(size_t)(off) = v; }
DEV v2u lds_tr(u32 off) {
  return __builtin_bit_cast(v2u, __builtin_amdgcn_ds_read_tr16_b64_v4i16((LAS s4v*)(size_t)(off)));
}
DEV v4u tr_frag(u32 a0, u32 a1) { v2u x = lds_tr(a0), y = lds_tr(a1); return v4u{x.x, x.y, y.x, y.y}; }
DEV f16v mfma32(v4u a, v4u b, f16v c) {
  return __builtin_amdgcn_mfma_f32_32x32x16_bf16(__builtin_bit_cast(bf8, a), __builtin_bit_cast(bf8, b), c, 0, 0, 0);
}
DEV f4 mfma16(v4u a, v4u b, f4 c) {
  return __builtin_amdgcn_mfma_f32_16x16x32_bf16(__builtin_bit_cast(bf8, a), __builtin_bit_cast(bf8, b), c, 0, 0, 0);
}
DEV f4 mfma16h(v4u a, v4u b, f4 c) {
  return __builtin_amdgcn_mfma_f32_16x16x32_f16(__builtin_bit_cast(h8, a), __builtin_bit_cast(h8, b), c, 0, 0, 0);
}

#define BAR() asm volatile("s_waitcnt lgkmcnt(0)\n\ts_barrier" ::: "memory")
#define VM0() asm volatile("s_waitcnt vmcnt(0)" ::: "memory")
#define LD128(dst, rs, vo, so, mods) \
  asm volatile("buffer_load_dwordx4 %0, %1, %2, %3 offen " mods : "=v"(dst) : "v"(vo), "s"(rs), "s"(so) : "memory")
#define LD64(dst, rs, vo, so, mods) \
  asm volatile("buffer_load_dwordx2 %0, %1, %2, %3 offen " mods : "=v"(dst) : "v"(vo), "s"(rs), "s"(so) : "memory")
#define LD16(dst, rs, vo, so, mods) \
  asm volatile("buffer_load_ushort %0, %1, %2, %3 offen " mods : "=v"(dst) : "v"(vo), "s"(rs), "s"(so) : "memory")
#define DMA(rs, vo, so, m, mods)                                                                          \
  asm volatile("s_mov_b32 m0, %3\n\ts_nop 0\n\tbuffer_load_dwordx4 %0, %1, %2 offen " mods " lds" ::"v"(vo), \
               "s"(rs), "s"(so), "s"(m)                                                                   \
               : "memory")
DEV void st128_dev(v4u v, rsrc_t rs, u32 vo, u32 so) {
  asm volatile("buffer_store_dwordx4 %0, %1, %2, %3 offen sc1\n\ts_nop 1" ::"v"(v), "v"(vo), "s"(rs), "s"(so) : "memory");
}
DEV void st64_dev(v2u v, rsrc_t rs, u32 vo, u32 so) { __builtin_amdgcn_raw_buffer_store_b64(v, rs, vo, so, 16); }
DEV bool tag_ok(v4u g, u32 ta, u32 tb) { return g.y == ta && g.w == tb; }

#define STAMP(i)                                                                                              \
  do {                                                                                                        \
    if ((i) == 0) asm volatile("s_memrealtime %0\n\ts_waitcnt lgkmcnt(0)" : "=s"(amp0)::"memory");           \
    if ((i) == AMP) asm volatile("s_memrealtime %0\n\ts_waitcnt lgkmcnt(0)" : "=s"(amp1)::"memory");         \
    if ((i) == 22 && w == 0) {                                                                                \
      u64 t0_, t1_;                                                                                           \
      asm volatile("s_memrealtime %0\n\ts_waitcnt lgkmcnt(0)" : "=s"(t0_)::"memory");                        \
      do { asm volatile("s_memrealtime %0\n\ts_waitcnt lgkmcnt(0)" : "=s"(t1_)::"memory"); } while (t1_ - t0_ < 10 * (amp1 - amp0)); \
    }                                                                                                         \
  } while (0)

DEV f2 cmul(f2 a, f2 b) { return f2{a.x * b.x - a.y * b.y, a.x * b.y + a.y * b.x}; }
DEV f2 cexpv(float al, float be, float v) {
  const float e = __builtin_amdgcn_exp2f(al * v);
  const float r = __builtin_amdgcn_fractf(be * v);
  return f2{e * __builtin_amdgcn_cosf(r), e * __builtin_amdgcn_sinf(r)};
}
DEV u32 kidx(int lag) {
  const u32 bits = pk((float)lag, 0.f) & 0xffffu;
  const int id = lag < 256 ? lag : (int)bits - 0x4380 + 256;
  return (u32)(lag < 0 ? 6 : 8 + 2 * id);
}

DEV void stage_w(rsrc_t rg, rsrc_t ru, u32 vo, int unit, int slot, int ii, int gj) {
  const u32 so = unit * (32 * 3584) + gj * 224;
  const u32 m = L_STG + slot * SLOT + ii * 1024;
  DMA(rg, vo, so, m, "");
  DMA(ru, vo, so, m + UPOFF, "");
}
DEV u32 stage_vo(int ii, int l) { const u32 u = ii * 64 + l, row = u / 14; return row * 3584 + (u - row * 14) * 16; }

__global__ void __launch_bounds__(512, 2) fused_kernel(Params p) {
  const int tid = threadIdx.x, l = tid & 63;
  const int w = __builtin_amdgcn_readfirstlane(tid >> 6);
  const int b = blockIdx.x, xcd = b & 7, r = b >> 3;
  const int cp = xcd * 32 + r;
  const int gi = (xcd >> 1) * 4 + (r >> 3), gj = (xcd & 1) * 8 + (r & 7);
  const bool ismod = r < 24;
  const int mgrp = xcd * 6 + (r >> 2), mkq = r & 3;
  const rsrc_t rws = mk(p.ws, WS_END);
  u64 amp0 = 0, amp1 = 0;
  STAMP(0);

  v2u nonce; v4u row, wm0, wm1; u32 pAr, pAi, pBr, pBi, pCr, pCi, c0, c1;
  {
    const u32 zero = 0;
    LD64(nonce, rws, zero, zero, "sc1");
    const rsrc_t rx = mk(p.x, kL * kD * 2);
    LD128(row, rx, l * 16, (b * 8 + w) * 1024, "nt");
    const u32 vo = (((w >> 1) & 1) * 64 + l) * 2, so = (2 * cp + (w >> 2)) * 256;
    LD16(pAr, mk(p.are, kD * kN * 2), vo, so, "nt");
    LD16(pAi, mk(p.aim, kD * kN * 2), vo, so, "nt");
    LD16(pBr, mk(p.bre, kD * kN * 2), vo, so, "nt");
    LD16(pBi, mk(p.bim, kD * kN * 2), vo, so, "nt");
    LD16(pCr, mk(p.cre, kD * kN * 2), vo, so, "nt");
    LD16(pCi, mk(p.cim, kD * kN * 2), vo, so, "nt");
    if (ismod) {
      const rsrc_t rc = mk(p.c, kD * 2), rm = mk(p.wmod, kD * 6 * kD * 2);
      LD16(c0, rc, (tid >> 3) * 2, mkq * 256, "");
      LD16(c1, rc, (tid >> 3) * 2, mkq * 256 + 128, "");
      const u32 vm = (tid >> 3) * 6144 + (tid & 7) * 16, sm = mkq * 128 * 6144 + mgrp * 128;
      LD128(wm0, rm, vm, sm, "nt");
      LD128(wm1, rm, vm, sm + 64 * 6144, "nt");
    }
  }
  const u32 ldt2 = ((const CAS u32*)p.logdt)[cp], dsk2 = ((const CAS u32*)p.dskip)[cp];
  u32 kix[2][7];
#pragma unroll
  for (int k = 0; k < 2; k++)
#pragma unroll
    for (int e = 0; e < 7; e++) kix[k][e] = L_G + (tid >> 8) * 1352 + kidx(4 * ((tid & 255) + 256 * k) + e);
  const u32 svo0 = stage_vo(w < 7 ? w : 0, l), svo1 = stage_vo(w < 3 ? w + 4 : 0, l);
  if (tid < 4) lds_st<u32>(M_CNT + tid * 4, 0u);
  if (tid < 2) lds_st<v2u>(L_G + tid * 1352, v2u{0u, 0u});

#pragma unroll
  for (int k = 0; k < 2; k++)
    asm volatile("" : "+v"(kix[k][0]), "+v"(kix[k][1]), "+v"(kix[k][2]), "+v"(kix[k][3]), "+v"(kix[k][4]), "+v"(kix[k][5]), "+v"(kix[k][6]));
  if (ismod) asm volatile("s_waitcnt vmcnt(4)" ::: "memory");
  else asm volatile("s_waitcnt vmcnt(0)" ::: "memory");
  asm volatile("" : "+v"(nonce), "+v"(row), "+v"(pAr), "+v"(pAi), "+v"(pBr), "+v"(pBi), "+v"(pCr), "+v"(pCi)::"memory");
  STAMP(1);
  const u64 tag64 = (((u64)(u32)__builtin_amdgcn_readfirstlane(nonce.y) << 32) | (u64)(u32)__builtin_amdgcn_readfirstlane(nonce.x)) + 0x9E3779B97F4A7C15ull;
  const u32 tagA = (u32)tag64, tagB = (u32)(tag64 >> 32);
  {
    lds_st<v4u>(L_ROW + w * 1040 + l * 16, row);
    float s = dot2(row.x, 0x3f803f80u, 0.f), q = dot2(row.x, row.x, 0.f);
    s = dot2(row.y, 0x3f803f80u, s); q = dot2(row.y, row.y, q);
    s = dot2(row.z, 0x3f803f80u, s); q = dot2(row.z, row.z, q);
    s = dot2(row.w, 0x3f803f80u, s); q = dot2(row.w, row.w, q);
    const float mean = wave_sum(s) * (1.f / kD);
    const float var = rbf(fmaxf(wave_sum(q) * (1.f / kD) - mean * mean, 0.f));
    const float den = __builtin_amdgcn_sqrtf(rbf(var + 1e-5f));
    if (l == 0) lds_st<u32>(M_RST + w * 4, pk(mean, den));
  }
  BAR();
  {
    const u32 a = L_ROW + (tid & 3) * 2080 + (tid >> 2) * 4;
    const u32 d0 = lds_ld<u32>(a), d1 = lds_ld<u32>(a + 1040), d2 = lds_ld<u32>(a + 512), d3 = lds_ld<u32>(a + 512 + 1040);
    st128_dev(v4u{d0, tagA, d1, tagB}, rws, tid * 16, WS_XG + b * 16384);
    st128_dev(v4u{d2, tagA, d3, tagB}, rws, tid * 16, WS_XG + b * 16384 + 8192);
  }
  if (w == 7 && l < 4) {
    const v2u s2 = lds_ld<v2u>(M_RST + l * 8);
    st128_dev(v4u{s2.x, tagA, s2.y, tagB}, rws, l * 16, WS_STG + b * 64);
  }
  const rsrc_t rwg = mk(p.wg, kD * kF * 2), rwu = mk(p.wu, kD * kF * 2);
  if (w < 7) {
#pragma unroll
    for (int u = 0; u < 5; u++) stage_w(rwg, rwu, svo0, u, u, w, gj);
  }
  STAMP(2);
  {
    const int ch = w >> 2, mode = ((w >> 1) & 1) * 64 + l;
    const float dt = rbf(expf(ch ? hi16(ldt2) : lo16(ldt2)));
    const float al = (dt * lo16(pAr)) * 1.44269504088896341f, be = (dt * lo16(pAi)) * 0.159154943091895336f;
    const u32 fsw = ((mode >> 1) & 1) << 2 | ((mode >> 2) & 1) << 1 | (mode & 1);
    const u32 base = L_FACT + ch * 32768 + (w & 1) * 16384 + mode * 128;
    if ((w & 1) == 0) {
      const f2 e8 = cexpv(al, be, 8.f);
      f2 v[4][8];
      v[0][0] = f2{1.f, 0.f};
      v[1][0] = cexpv(al, be, 64.f); v[2][0] = cexpv(al, be, 128.f); v[3][0] = cexpv(al, be, 192.f);
#pragma unroll
      for (int i = 1; i < 8; i++)
#pragma unroll
        for (int c = 0; c < 4; c++) v[c][i] = cmul(v[c][i - 1], e8);
#pragma unroll
      for (int c = 0; c < 4; c++) {
        lds_st<v4u>(base + ((c ^ fsw) << 4), v4u{pkh(v[c][0].x, v[c][1].x), pkh(v[c][2].x, v[c][3].x), pkh(v[c][4].x, v[c][5].x), pkh(v[c][6].x, v[c][7].x)});
        lds_st<v4u>(base + (((4 + c) ^ fsw) << 4), v4u{pkh(v[c][0].y, v[c][1].y), pkh(v[c][2].y, v[c][3].y), pkh(v[c][4].y, v[c][5].y), pkh(v[c][6].y, v[c][7].y)});
      }
    } else {
      const f2 e1 = cexpv(al, be, 1.f), e256 = cexpv(al, be, 256.f);
      f2 wv = cmul(f2{lo16(pCr), lo16(pCi)}, f2{dt * lo16(pBr), dt * lo16(pBi)});
      u32 ex = wave_umax(umax(ubits(wv.x) & 0x7fffffffu, ubits(wv.y) & 0x7fffffffu)) >> 23;
      ex = ex < 14 ? 14 : (ex > 254 ? 254 : ex);
      wv *= fbits((267 - ex) << 23);
      if (l == 0) lds_st<u32>(M_INV + (w >> 1) * 4, (ex - 13) << 23);
      const f2 e2 = cmul(e1, e1), e4 = cmul(e2, e2);
      f2 v[24];
      v[0] = wv;
#pragma unroll
      for (int i = 1; i < 8; i++) v[i] = cmul(v[i - 1], e1);
      v[8] = cmul(wv, e256);
      v[12] = cmul(v[8], e256); v[14] = cmul(v[12], e256); v[16] = cmul(v[14], e256);
      v[9] = cmul(v[8], e2); v[10] = cmul(v[8], e4); v[11] = cmul(v[10], e2);
      v[13] = cmul(v[12], e4); v[15] = cmul(v[14], e4);
#pragma unroll
      for (int i = 17; i < 21; i++) v[i] = cmul(v[i - 1], e256);
      v[21] = v[22] = v[23] = f2{0.f, 0.f};
#pragma unroll
      for (int c = 0; c < 3; c++) {
        const f2* q = v + 8 * c;
        lds_st<v4u>(base + ((c ^ fsw) << 4), v4u{pkh(q[0].x, q[1].x), pkh(q[2].x, q[3].x), pkh(q[4].x, q[5].x), pkh(q[6].x, q[7].x)});
        lds_st<v4u>(base + (((4 + c) ^ fsw) << 4), v4u{pkh(-q[0].y, -q[1].y), pkh(-q[2].y, -q[3].y), pkh(-q[4].y, -q[5].y), pkh(-q[6].y, -q[7].y)});
      }
    }
  }
  STAMP(3);
  if (ismod) {
    if (w < 7) asm volatile("s_waitcnt vmcnt(12)" ::: "memory");
    else asm volatile("s_waitcnt vmcnt(3)" ::: "memory");
    asm volatile("" : "+v"(c0), "+v"(c1), "+v"(wm0), "+v"(wm1)::"memory");
    const float ca = lo16(c0), cb = lo16(c1);
    const float sa = rbf(ca / (1.f + expf(-ca))), sb = rbf(cb / (1.f + expf(-cb)));
    float a[8];
#pragma unroll
    for (int e = 0; e < 4; e++) {
      a[2 * e] = sa * lo16(wm0[e]) + sb * lo16(wm1[e]);
      a[2 * e + 1] = sa * hi16(wm0[e]) + sb * hi16(wm1[e]);
    }
    float t[4], u[2];
#pragma unroll
    for (int e = 0; e < 4; e++) {
      auto s = __builtin_amdgcn_permlane32_swap(ubits(a[e]), ubits(a[e + 4]), false, false);
      t[e] = fbits(s[0]) + fbits(s[1]);
    }
#pragma unroll
    for (int e = 0; e < 2; e++) {
      auto s = __builtin_amdgcn_permlane16_swap(ubits(t[e]), ubits(t[e + 2]), false, false);
      u[e] = fbits(s[0]) + fbits(s[1]);
      u[e] += dppf<0x128>(u[e]);
    }
    if ((l & 8) == 0) lds_st<f2>(L_MODP + w * 256 + ((l & 7) * 8 + (l >> 5) * 4 + ((l >> 4) & 1) * 2) * 4, f2{u[0], u[1]});
  }
  BAR();
  if (ismod && w == 7 && l < 32) {
    f2 s = lds_ld<f2>(L_MODP + l * 8);
#pragma unroll
    for (int k = 1; k < 8; k++) s += lds_ld<f2>(L_MODP + k * 256 + l * 8);
    st128_dev(v4u{ubits(s.x), tagA, ubits(s.y), tagB}, rws, l * 16, WS_MODG + mkq * 24576 + mgrp * 512);
  }
  {
    const int ch = w >> 2, mt = (w >> 1) & 1, nt = w & 1, q = l >> 4, rq = (l >> 2) & 3, pp = l & 3;
    f4 acc[2] = {f4{0, 0, 0, 0}, f4{0, 0, 0, 0}};
    u32 aa[2], ab[2];
#pragma unroll
    for (int rd = 0; rd < 2; rd++) {
      const u32 m = 4 * q + 2 * rd + (rq >> 1);
      const u32 fsw = ((m >> 1) & 1) << 2 | ((m >> 2) & 1) << 1 | (m & 1);
      const u32 base = L_FACT + ch * 32768 + m * 128 + (pp & 1) * 8;
      aa[rd] = base + 16384 + ((((rq & 1) * 4 + mt * 2 + (pp >> 1)) ^ fsw) << 4);
      ab[rd] = base + ((((rq & 1) * 4 + nt * 2 + (pp >> 1)) ^ fsw) << 4);
    }
#pragma unroll
    for (int ks = 0; ks < 8; ks++)
      acc[ks >> 2] = mfma16h(tr_frag(aa[0] + ks * 2048, aa[1] + ks * 2048), tr_frag(ab[0] + ks * 2048, ab[1] + ks * 2048), acc[ks >> 2]);
    const f2 inv = lds_ld<f2>(M_INV + ch * 8);
    const f4 kv = acc[0] * inv.x + acc[1] * inv.y;
    const u32 p01 = pk(kv.x, kv.y), p23 = pk(kv.z, kv.w), bp = 16 * nt + (l & 15), g = L_G + ch * 1352 + 8;
    if (mt == 0) {
      if (q < 3) lds_st<v2u>(g + (q < 2 ? 8 * bp + 4 * q : 256 + 4 * bp) * 2, v2u{p01, p23});
      else { lds_st<u32>(g + (384 + 2 * bp) * 2, p01); lds_st<u32>(g + (448 + 2 * bp) * 2, p23); }
    } else if (q == 0) {
      lds_st<u16>(g + (512 + bp) * 2, (u16)p01); lds_st<u16>(g + (544 + bp) * 2, (u16)(p01 >> 16));
      lds_st<u16>(g + (576 + bp) * 2, (u16)p23); lds_st<u16>(g + (608 + bp) * 2, (u16)(p23 >> 16));
    } else if (q == 1) lds_st<u16>(g + (640 + bp) * 2, (u16)p01);
  }
  BAR();
  STAMP(4);
  v4u gx0, gx1, gs0, gs1, gm;
  const u32 vgx = (tid >> 1) * 16384 + (tid & 1) * 32, lm = l < 12 ? l : 11;
  const u32 vgm = (lm & 3) * 24576 + (lm >> 2) * 4096;
  {
#pragma unroll
    for (int k = 0; k < 2; k++) {
      u32 v[7];
#pragma unroll
      for (int e = 0; e < 7; e++) v[e] = lds_ld<u16>(kix[k][e]);
      const u32 a = L_KC + (tid >> 8) * 16640 + ((tid & 255) + 256 * k + 4) * 8;
#pragma unroll
      for (int s = 0; s < 4; s++) lds_st<v2u>(a + s * 4160, v2u{v[s] | v[s + 1] << 16, v[s + 2] | v[s + 3] << 16});
    }
    if (tid < 8) {
      u32 v[7];
#pragma unroll
      for (int e = 0; e < 7; e++) v[e] = lds_ld<u16>(L_G + (tid >> 2) * 1352 + kidx(4 * (tid & 3) - 16 + e));
      const u32 a = L_KC + (tid >> 2) * 16640 + (tid & 3) * 8;
#pragma unroll
      for (int s = 0; s < 4; s++) lds_st<v2u>(a + s * 4160, v2u{v[s] | v[s + 1] << 16, v[s + 2] | v[s + 3] << 16});
    }
    if (tid < 64) lds_st<v4u>(L_HR + (tid >> 5) * 4608 + 4096 + (tid & 31) * 16, v4u{0u, 0u, 0u, 0u});
  }
  STAMP(5);
  for (u32 spin = 0;; spin++) {
    LD128(gx0, rws, vgx, WS_XG + cp * 64, "sc1");
    LD128(gx1, rws, vgx, WS_XG + cp * 64 + 16, "sc1");
    LD128(gs0, rws, tid * 32, WS_STG, "sc1");
    LD128(gs1, rws, tid * 32, WS_STG + 16, "sc1");
    LD128(gm, rws, vgm, WS_MODG + cp * 16, "sc1");
    asm volatile("s_waitcnt vmcnt(0)" : "+v"(gx0), "+v"(gx1), "+v"(gs0), "+v"(gs1), "+v"(gm)::"memory");
    const bool ok = tag_ok(gx0, tagA, tagB) && tag_ok(gx1, tagA, tagB) && tag_ok(gs0, tagA, tagB) && tag_ok(gs1, tagA, tagB) && tag_ok(gm, tagA, tagB);
    if (__all(ok) || spin > kSpin) break;
    __builtin_amdgcn_s_sleep(4);
  }
  STAMP(6);
  float ga0, ga1;
  {
    float m0 = fbits(gm.x), m1 = fbits(gm.z);
    m0 += dppf<0xB1>(m0); m1 += dppf<0xB1>(m1);
    m0 += dppf<0x4E>(m0); m1 += dppf<0x4E>(m1);
    const u32 mm = pk(m0, m1);
    const u32 msa = __builtin_amdgcn_readlane(mm, 0), mca = __builtin_amdgcn_readlane(mm, 4), mga = __builtin_amdgcn_readlane(mm, 8);
    const float sa0 = lo16(msa), sa1 = hi16(msa), s0 = rbf(1.f + lo16(mca)), s1 = rbf(1.f + hi16(mca));
    ga0 = lo16(mga); ga1 = hi16(mga);
    const u32 xs[4] = {gx0.x, gx0.z, gx1.x, gx1.z}, st[4] = {gs0.x, gs0.z, gs1.x, gs1.z};
    float h0[4], h1[4];
#pragma unroll
    for (int e = 0; e < 4; e++) {
      const float m = lo16(st[e]), rd = __builtin_amdgcn_rcpf(hi16(st[e]));
      const u32 xn = pk((lo16(xs[e]) - m) * rd, (hi16(xs[e]) - m) * rd);
      h0[e] = lo16(xn) * s0 + sa0; h1[e] = hi16(xn) * s1 + sa1;
    }
    lds_st<v2u>(L_HR + (2044 - 4 * tid) * 2, v2u{pk(h0[3], h0[2]), pk(h0[1], h0[0])});
    lds_st<v2u>(L_HR + 4608 + (2044 - 4 * tid) * 2, v2u{pk(h1[3], h1[2]), pk(h1[1], h1[0])});
    lds_st<v2u>(L_XB + tid * 8, v2u{(xs[0] & 0xffffu) | xs[1] << 16, (xs[2] & 0xffffu) | xs[3] << 16});
    lds_st<v2u>(L_XB + 4096 + tid * 8, v2u{xs[0] >> 16 | (xs[1] & 0xffff0000u), xs[2] >> 16 | (xs[3] & 0xffff0000u)});
  }
  BAR();
  STAMP(7);
  if (b == 0 && tid == 0) st64_dev(v2u{tagA, tagB}, rws, 0, WS_NONCE);

  v4u W[32];
  const u32 wtr = ((l >> 5) * 8 + ((l >> 2) & 3)) * 224 + ((l >> 1) & 1) * UPOFF + (w * 16 + ((l >> 4) & 1) * 8 + (l & 1) * 4) * 2 + L_STG;
#define READ_W(u0, n)                                                                            \
  _Pragma("unroll") for (int u = 0; u < (n); u++) _Pragma("unroll") for (int k = 0; k < 2; k++)  \
      W[2 * ((u0) + u) + k] = tr_frag(wtr + u * SLOT + k * 3584, wtr + u * SLOT + k * 3584 + 896)
  if (w < 7) { READ_W(0, 5); }
  const int t4 = tid & 255;
  const int ech = t4 >> 7;
  const float gav = ech ? ga1 : ga0, dsk = ech ? hi16(dsk2) : lo16(dsk2);
  auto conv_epilogue = [&](int half) {
    const int tile = (t4 >> 5) & 3, n = (t4 >> 1) & 15, qp = t4 & 1, t0 = 1024 * half + 256 * tile + 16 * n + 8 * qp;
    const u32 pa = L_P + ech * 4096 + tile * 1024 + (n + 32 * qp) * 16;
    const f4 y0 = lds_ld<f4>(pa) + lds_ld<f4>(pa + 8192), y1 = lds_ld<f4>(pa + 256) + lds_ld<f4>(pa + 8192 + 256);
    const v4u hv = lds_ld<v4u>(L_HR + ech * 4608 + (2040 - t0) * 2), xv = lds_ld<v4u>(L_XB + ech * 4096 + t0 * 2);
    const float y[8] = {y0.x, y0.y, y0.z, y0.w, y1.x, y1.y, y1.z, y1.w};
    v4u o;
#pragma unroll
    for (int e = 0; e < 4; e++) {
      const u32 yr = pk(y[2 * e], y[2 * e + 1]), hd = pk(hi16(hv[3 - e]) * dsk, lo16(hv[3 - e]) * dsk);
      const u32 y2 = pk(lo16(yr) + lo16(hd), hi16(yr) + hi16(hd));
      const u32 z = pk(gav * lo16(y2), gav * hi16(y2));
      o[e] = pk(lo16(xv[e]) + lo16(z), hi16(xv[e]) + hi16(z));
    }
    st128_dev(o, rws, (t0 >> 6) * 65536 + (t0 & 63) * 2 + ech * 128, WS_X1T + cp * 256);
  };
  auto publish_x1 = [&](int half) {
    u32 old = 0;
    if (l == 0) old = __hip_atomic_fetch_add((LAS u32*)(size_t)(M_CNT + half * 4), 1u, __ATOMIC_RELAXED, __HIP_MEMORY_SCOPE_WORKGROUP);
    if (__builtin_amdgcn_readfirstlane(old) == 3 && l < 8) st64_dev(v2u{tagA, tagB}, rws, l * 8192, WS_F2 + half * 65536 + b * 32);
  };
  if (w < 4) {
    const int ch = w & 1, bh = w >> 1, m = l & 15, q = l >> 4;
    const int lag0 = 16 * (q >> 1) + m - 8 * (q & 1) - 7, sg = (m + 1) & 3;
    const u32 ka = L_KC + ch * 16640 + sg * 4160 + (lag0 - sg + 16) * 2 + bh * 256;
    const u32 ha = L_HR + ch * 4608 + 4080 - 3584 - 32 * m + 32 * (q >> 1) - 16 * (q & 1) + bh * 256;
    auto ldA = [&](int a, int bb) { const v2u x = lds_ld<v2u>(ka + 512 * a + 64 * bb), y = lds_ld<v2u>(ka + 512 * a + 64 * bb + 8); return v4u{x.x, x.y, y.x, y.y}; };
    auto ldB = [&](int i, int bb) { return lds_ld<v4u>(ha + 512 * (7 - i) + 64 * bb); };
    f4 acc[4];
#pragma unroll
    for (int i = 0; i < 4; i++) acc[i] = f4{0, 0, 0, 0};
#pragma unroll
    for (int bb = 0; bb < 4; bb++) {
      v4u A[4], B[4];
#pragma unroll
      for (int i = 0; i < 4; i++) { A[i] = ldA(i, bb); B[i] = ldB(i, bb); }
#pragma unroll
      for (int a = 0; a < 4; a++)
#pragma unroll
        for (int i = a; i < 4; i++) acc[i] = mfma16(A[a], B[i - a], acc[i]);
    }
#pragma unroll
    for (int i = 0; i < 4; i++) lds_st<f4>(L_P + w * 4096 + i * 1024 + l * 16, acc[i]);
    BAR();
    STAMP(8);
#pragma unroll
    for (int u = 0; u < 6; u++) {
      stage_w(rwg, rwu, svo0, 5 + u, u, w, gj);
      if (w < 3) stage_w(rwg, rwu, svo1, 5 + u, u, w + 4, gj);
    }
#pragma unroll
    for (int i = 0; i < 4; i++) acc[i] = f4{0, 0, 0, 0};
#pragma unroll
    for (int bb = 0; bb < 4; bb++) {
      v4u B[8];
#pragma unroll
      for (int i = 0; i < 8; i++) B[i] = ldB(i, bb);
#pragma unroll
      for (int a = 0; a < 8; a++) {
        const v4u A = ldA(a, bb);
#pragma unroll
        for (int i = (a > 4 ? a : 4); i < 8; i++) acc[i - 4] = mfma16(A, B[i - a], acc[i - 4]);
      }
    }
    STAMP(9);
    BAR();
#pragma unroll
    for (int i = 0; i < 4; i++) lds_st<f4>(L_P + w * 4096 + i * 1024 + l * 16, acc[i]);
    VM0();
    BAR();
  } else {
    BAR();
    STAMP(8);
    conv_epilogue(0);
    VM0();
    publish_x1(0);
    v4u g[9];
    const u32 tg = t4 < 128 ? t4 : 127;
    for (u32 spin = 0;; spin++) {
#pragma unroll
      for (int k = 0; k < 4; k++) {
        LD128(g[k], rws, t4 * 16, WS_MODG + k * 24576 + 768 * 16, "sc1");
        LD128(g[4 + k], rws, t4 * 16, WS_MODG + k * 24576 + 1024 * 16, "sc1");
      }
      LD128(g[8], rws, (tg & 3) * 24576 + (tg >> 2) * 16, WS_MODG + (1280 + (gj & 7) * 32) * 16, "sc1");
      asm volatile("s_waitcnt vmcnt(0)" : "+v"(g[0]), "+v"(g[1]), "+v"(g[2]), "+v"(g[3]), "+v"(g[4]), "+v"(g[5]), "+v"(g[6]), "+v"(g[7]), "+v"(g[8])::"memory");
      bool ok = true;
#pragma unroll
      for (int k = 0; k < 9; k++) ok = ok && tag_ok(g[k], tagA, tagB);
      if (__all(ok) || spin > kSpin) break;
      __builtin_amdgcn_s_sleep(4);
    }
    const u32 sbp = pk((fbits(g[0].x) + fbits(g[1].x)) + (fbits(g[2].x) + fbits(g[3].x)), (fbits(g[0].z) + fbits(g[1].z)) + (fbits(g[2].z) + fbits(g[3].z)));
    const u32 cbp = pk((fbits(g[4].x) + fbits(g[5].x)) + (fbits(g[6].x) + fbits(g[7].x)), (fbits(g[4].z) + fbits(g[5].z)) + (fbits(g[6].z) + fbits(g[7].z)));
    lds_st<u32>(L_V + t4 * 4, pk(1.f + lo16(cbp), 1.f + hi16(cbp)));
    lds_st<u32>(L_V + 1024 + t4 * 4, sbp);
    float m0 = fbits(g[8].x), m1 = fbits(g[8].z);
    m0 += dppf<0xB1>(m0); m1 += dppf<0xB1>(m1);
    m0 += dppf<0x4E>(m0); m1 += dppf<0x4E>(m1);
    if (t4 < 128 && (t4 & 3) == 0) lds_st<f2>(L_GB + (t4 >> 2) * 8, f2{rbf(m0), rbf(m1)});
    STAMP(9);
    BAR();
    BAR();
    conv_epilogue(1);
  }
  STAMP(10);
  if (w < 7) { READ_W(5, 6); }
  BAR();
  if (w < 4) {
#pragma unroll
    for (int u = 0; u < 5; u++) {
      stage_w(rwg, rwu, svo0, 11 + u, u, w, gj);
      if (w < 3) stage_w(rwg, rwu, svo1, 11 + u, u, w + 4, gj);
    }
  }
  {
    const u32 sv = lds_ld<u16>(L_V + tid * 2), e = tid & 7, sh = sv << ((e & 1) * 16);
    lds_st<v4u>(L_DIAG + (tid >> 4) * 336 + (tid & 15) * 16, v4u{(e >> 1) == 0 ? sh : 0u, (e >> 1) == 1 ? sh : 0u, (e >> 1) == 2 ? sh : 0u, (e >> 1) == 3 ? sh : 0u});
    if (tid < 128) lds_st<v4u>(L_DIAG + (tid >> 2) * 336 + 256 + (tid & 3) * 16, lds_ld<v4u>(L_V + ((tid >> 1) & 1) * 1024 + (tid >> 2) * 32 + (tid & 1) * 16));
    if (tid < 32) lds_st<v4u>(L_DIAG + tid * 336 + 320, v4u{0u, 0u, 0u, 0u});
  }
  auto wait_x1 = [&](int half) {
    for (u32 spin = 0;; spin++) {
      v2u f0, f1, f2_, f3;
      const u32 so = WS_F2 + half * 65536 + xcd * 8192;
      LD64(f0, rws, l * 32, so, "sc1");
      LD64(f1, rws, l * 32, so + 2048, "sc1");
      LD64(f2_, rws, l * 32, so + 4096, "sc1");
      LD64(f3, rws, l * 32, so + 6144, "sc1");
      asm volatile("s_waitcnt vmcnt(0)" : "+v"(f0), "+v"(f1), "+v"(f2_), "+v"(f3)::"memory");
      const bool ok = f0.x == tagA && f0.y == tagB && f1.x == tagA && f1.y == tagB && f2_.x == tagA && f2_.y == tagB && f3.x == tagA && f3.y == tagB;
      if (__all(ok) || spin > kSpin) break;
      __builtin_amdgcn_s_sleep(4);
    }
  };
  if (w >= 4) { VM0(); publish_x1(1); }
  BAR();
  STAMP(11);
  f2 stv = {0.f, 0.f};
  const u32 dga = L_DIAG + ((l & 31) < 16 ? (((l >> 3) & 1) == (l >> 5) ? (l & 15) * 16 : 320) : ((l & 31) < 18 ? 256 + (l & 1) * 32 + (l >> 5) * 16 : 320));
#define PREP_W(s0, s1)                                                                       \
  _Pragma("unroll") for (int s = (s0); s < (s1); s++) {                                      \
    const f16v d = mfma32(lds_ld<v4u>(dga + s * 336), W[s], f16v{});                         \
    W[s] = v4u{pk(d[0], d[1]), pk(d[2], d[3]), pk(d[4], d[5]), pk(d[6], d[7])};              \
    stv += f2{d[8], d[9]};                                                                   \
  }
  const u32 tvo = (l >> 3) * 128 + (((l & 7) ^ (((l >> 4) & 1) << 2)) << 4);
  if (w < 7) { PREP_W(0, 22); VM0(); }
  else wait_x1(0);
  STAMP(12);
  BAR();
#pragma unroll
  for (int k = 0; k < 8; k++) DMA(rws, tvo, WS_X1T + gi * 65536 + (w * 8 + k) * 1024, L_TA + (w * 8 + k) * 1024, "sc1");
  if (w < 7) {
    READ_W(11, 5);
    PREP_W(22, 32);
    if (l < 32) {
      lds_st<float>(L_ST + w * 256 + l * 4, stv.x);
      lds_st<float>(L_ST + w * 256 + 128 + l * 4, (l & 8) ? stv.y : stv.y * -1.44269504088896341f);
    }
  }
  STAMP(13);
  VM0();
  BAR();
  STAMP(14);

  const u32 tra = ((l >> 5) * 4 + ((l >> 2) & 3)) * 128 + ((((l >> 4) & 1) * 32 + (l & 3) * 8) ^ (((l >> 3) & 1) << 6));
  const u32 rra = ((gj & 7) * 64 + (w >> 1) * 16 + (l >> 4) * 4 + ((l >> 2) & 3)) * 128 + (((gj >> 3) * 64 + (w & 1) * 32 + (l & 3) * 8) ^ (((l >> 3) & 1) << 6));
  const rsrc_t rwd = mk(p.wd, kF * kD * 2);
  const u32 dvo = (l >> 3) * 1024 + (((l & 7) ^ (((l >> 4) & 1) << 2)) << 4);
  const u32 d1base = w < 6 ? L_X + w * 10240 : 138752 + (w - 6) * 10240;
  f16v acc[2];
  auto gemm1 = [&](u32 tb, int s0, int s1) {
#pragma unroll
    for (int s = 0; s < 32; s++)
      if (s >= s0 && s < s1) {
        acc[0] = mfma32(W[s], tr_frag(tb + tra + s * 2048, tb + tra + s * 2048 + 1024), acc[0]);
        acc[1] = mfma32(W[s], tr_frag(tb + (tra ^ 64) + s * 2048, tb + (tra ^ 64) + s * 2048 + 1024), acc[1]);
      }
  };
  auto ln2_stats = [&](u32 tb, int half) {
#pragma unroll
    for (int nt = 0; nt < 2; nt++) {
      float s = 0.f, q = 0.f, s2 = 0.f, q2 = 0.f;
      const u32 a = tb + (tra ^ (nt * 64));
#pragma unroll 8
      for (int k = 0; k < 32; k++) {
        const v2u x = lds_tr(a + k * 2048), y = lds_tr(a + k * 2048 + 1024);
        s = dot2(x.x, 0x3f803f80u, s); q = dot2(x.x, x.x, q); s2 = dot2(x.y, 0x3f803f80u, s2); q2 = dot2(x.y, x.y, q2);
        s = dot2(y.x, 0x3f803f80u, s); q = dot2(y.x, y.x, q); s2 = dot2(y.y, 0x3f803f80u, s2); q2 = dot2(y.y, y.y, q2);
      }
      auto sw = __builtin_amdgcn_permlane32_swap(ubits(s + s2), ubits(q + q2), false, false);
      const float t = fbits(sw[0]) + fbits(sw[1]);
      auto s3 = __builtin_amdgcn_permlane32_swap(ubits(t), ubits(t), false, false);
      const float mean = fbits(s3[0]) * (1.f / kD);
      const float var = rbf(fmaxf(fbits(s3[1]) * (1.f / kD) - mean * mean, 0.f));
      const float den = rbf(__builtin_amdgcn_sqrtf(rbf(var + 1e-5f)));
      if (l < 32) lds_st<f2>(L_STAT + half * 512 + (nt * 32 + l) * 8, f2{rbf(mean), __builtin_amdgcn_rcpf(den)});
    }
  };
  auto glu_epilogue = [&](int half) {
    f4 S[4], T[4];
#pragma unroll
    for (int k = 0; k < 4; k++) {
      S[k] = lds_ld<f4>(L_ST + w * 256 + k * 32 + (l >> 5) * 16);
      T[k] = lds_ld<f4>(L_ST + w * 256 + 128 + k * 32 + (l >> 5) * 16);
    }
#pragma unroll
    for (int nt = 0; nt < 2; nt++) {
      const f2 st = lds_ld<f2>(L_STAT + half * 512 + (nt * 32 + (l & 31)) * 8);
      const float rg = st.y * -1.44269504088896341f;
      float o[8];
#pragma unroll
      for (int k = 0; k < 2; k++)
#pragma unroll
        for (int e = 0; e < 4; e++) {
          const float z = (acc[nt][8 * k + e] - st.x * S[2 * k][e]) * rg + T[2 * k][e];
          const float u = (acc[nt][8 * k + 4 + e] - st.x * S[2 * k + 1][e]) * st.y + T[2 * k + 1][e];
          o[4 * k + e] = u * __builtin_amdgcn_rcpf(1.f + __builtin_amdgcn_exp2f(z));
        }
      st128_dev(v4u{pk(o[0], o[1]), pk(o[2], o[3]), pk(o[4], o[5]), pk(o[6], o[7])}, rws, l * 16,
                WS_HH + ((half * 32 + 2 * gi + nt) * 112 + 7 * gj + w) * 1024);
    }
  };
  auto publish_hh = [&](int half) {
    u32 old = 0;
    if (l == 0) old = __hip_atomic_fetch_add((LAS u32*)(size_t)(M_CNT + 8 + half * 4), 1u, __ATOMIC_RELAXED, __HIP_MEMORY_SCOPE_WORKGROUP);
    if (__builtin_amdgcn_readfirstlane(old) == 6 && l == 0) st64_dev(v2u{tagA, tagB}, rws, 0, WS_F3 + ((half * 16 + gi) * 16 + gj) * 128);
  };
  auto stage_d = [&](int s0, int n, u32 base) {
#pragma unroll
    for (int k = 0; k < 2 * n; k++) DMA(rwd, dvo, (w * 224 + s0 * 16 + k * 8) * 1024 + (gj & 7) * 128, base + k * 1024, "");
  };
  const v2u resa = lds_tr(L_TA + rra);
  if (w < 7) {
    acc[0] = acc[1] = f16v{};
    gemm1(L_TA, 0, 32);
  } else {
    wait_x1(1);
#pragma unroll 8
    for (int k = 0; k < 64; k++) DMA(rws, tvo, WS_X1T + (16 + gi) * 65536 + k * 1024, L_TB + k * 1024, "sc1");
    ln2_stats(L_TA, 0);
    VM0();
  }
  STAMP(15);
  BAR();
  const v2u resb = lds_tr(L_TB + rra);
  if (w < 7) {
    glu_epilogue(0);
    acc[0] = acc[1] = f16v{};
    gemm1(L_TB, 0, 8);
    VM0();
    publish_hh(0);
    stage_d(0, 5, d1base);
    gemm1(L_TB, 8, 32);
  } else {
    stage_d(0, 5, d1base);
    ln2_stats(L_TB, 1);
  }
  STAMP(16);
  BAR();
  if (w < 7) glu_epilogue(1);
  VM0();
  if (w < 7) publish_hh(1);
  STAMP(17);

  v4u Wd[14][2];
#pragma unroll
  for (int s = 0; s < 5; s++)
#pragma unroll
    for (int mt = 0; mt < 2; mt++) Wd[s][mt] = tr_frag(d1base + (tra ^ (mt * 64)) + s * 2048, d1base + (tra ^ (mt * 64)) + s * 2048 + 1024);
  BAR();
  const u32 d2base = L_X + w * 18432;
  stage_d(5, 9, d2base);
  v4u hf[14];
  auto load_hh = [&](int half) {
    for (u32 spin = 0;; spin++) {
      v2u f;
      LD64(f, rws, (l & 1) * 128, WS_F3 + ((half * 16 + gi) * 16 + 2 * w) * 128, "sc1");
      asm volatile("s_waitcnt vmcnt(0)" : "+v"(f)::"memory");
      if (__all(f.x == tagA && f.y == tagB) || spin > kSpin) break;
      __builtin_amdgcn_s_sleep(4);
    }
#pragma unroll
    for (int s = 0; s < 14; s++)
      hf[s] = __builtin_amdgcn_raw_buffer_load_b128(rws, l * 16 + (s & 3) * 1024, WS_HH + ((half * 32 + 2 * gi + (gj >> 3)) * 112 + 14 * w + (s & ~3)) * 1024, 16);
  };
  auto gemm2 = [&](u32 red) {
    acc[0] = acc[1] = f16v{};
#pragma unroll
    for (int s = 0; s < 14; s++) {
      acc[0] = mfma32(Wd[s][0], hf[s], acc[0]);
      acc[1] = mfma32(Wd[s][1], hf[s], acc[1]);
    }
#pragma unroll
    for (int mt = 0; mt < 2; mt++)
#pragma unroll
      for (int g = 0; g < 4; g++)
        lds_st<f4>(red + w * 8192 + (mt * 4 + g) * 1024 + l * 16, f4{acc[mt][4 * g], acc[mt][4 * g + 1], acc[mt][4 * g + 2], acc[mt][4 * g + 3]});
  };
  const rsrc_t rout = mk(p.out, kL * kD * 2);
  auto out_epilogue = [&](int half, u32 red, v2u res) {
    const u32 a = red + ((w >> 1) * 4 + (l >> 4)) * 512 + ((w & 1) * 16 + (l & 15)) * 16;
    f4 s = lds_ld<f4>(a);
#pragma unroll
    for (int k = 1; k < 8; k++) s += lds_ld<f4>(a + k * 8192);
    const f4 gb = lds_ld<f4>(L_GB + ((w >> 1) * 4 + (l >> 4)) * 16);
    const u32 r01 = pk(s.x, s.y), r23 = pk(s.z, s.w);
    const u32 z01 = pk(gb.x * lo16(r01), gb.y * hi16(r01)), z23 = pk(gb.z * lo16(r23), gb.w * hi16(r23));
    const v2u o = {pk(lo16(res.x) + lo16(z01), hi16(res.x) + hi16(z01)), pk(lo16(res.y) + lo16(z23), hi16(res.y) + hi16(z23))};
    __builtin_amdgcn_raw_buffer_store_b64(o, rout, ((w & 1) * 16 + (l & 15)) * 1024 + ((w >> 1) * 4 + (l >> 4)) * 8,
                                          (half * 1024 + gi * 64 + (gj >> 3) * 32) * 1024 + (gj & 7) * 128, 0);
  };
  load_hh(0);
  STAMP(18);
#pragma unroll
  for (int s = 5; s < 14; s++)
#pragma unroll
    for (int mt = 0; mt < 2; mt++) Wd[s][mt] = tr_frag(d2base + (tra ^ (mt * 64)) + (s - 5) * 2048, d2base + (tra ^ (mt * 64)) + (s - 5) * 2048 + 1024);
  BAR();
  gemm2(L_X);
  STAMP(19);
  load_hh(1);
  STAMP(20);
  BAR();
  out_epilogue(0, L_X, resa);
  gemm2(L_STG);
  STAMP(21);
  BAR();
  out_epilogue(1, L_STG, resb);
  STAMP(22);
}

}

extern "C" void kernel_launch(void* const* d_in, const int* in_sizes, int n_in, void* d_out, int out_size, void* d_ws,
                              size_t ws_size, hipStream_t stream) {
  using namespace s4glu;
  (void)in_sizes; (void)n_in; (void)out_size; (void)ws_size;
  Params p;
  p.x = d_in[0]; p.are = d_in[1]; p.aim = d_in[2]; p.bre = d_in[3]; p.bim = d_in[4]; p.cre = d_in[5]; p.cim = d_in[6];
  p.logdt = d_in[7]; p.dskip = d_in[8]; p.wg = d_in[10]; p.wu = d_in[11]; p.wd = d_in[12]; p.c = d_in[13]; p.wmod = d_in[14];
  p.out = d_out; p.ws = d_ws;
  (void)hipFuncSetAttribute((const void*)fused_kernel, hipFuncAttributeMaxDynamicSharedMemorySize, L_END);
  fused_kernel<<<256, 512, L_END, stream>>>(p);
}
```

```cpp
#include <hip/hip_runtime.h>
#include <stdint.h>

#define DBG 0

namespace s4glu {

typedef unsigned short u16;
typedef unsigned u32;
typedef unsigned long long u64;
typedef u32 v4u __attribute__((ext_vector_type(4)));
typedef u32 v2u __attribute__((ext_vector_type(2)));
typedef float f2 __attribute__((ext_vector_type(2)));
typedef float f4 __attribute__((ext_vector_type(4)));
typedef float f16v __attribute__((ext_vector_type(16)));
typedef __bf16 bf2 __attribute__((ext_vector_type(2)));
typedef __bf16 bf8 __attribute__((ext_vector_type(8)));
typedef _Float16 h2 __attribute__((ext_vector_type(2)));
typedef _Float16 h8 __attribute__((ext_vector_type(8)));
typedef short s4v __attribute__((ext_vector_type(4)));
typedef __amdgpu_buffer_rsrc_t rsrc_t;
#define LAS __attribute__((address_space(3)))
#define CAS __attribute__((address_space(4)))
#define DEV __device__ __forceinline__

constexpr int kL = 2048, kD = 512, kN = 128, kF = 1792;

constexpr u32 WS_NONCE = 0;
constexpr u32 WS_STG = 4096;
constexpr u32 WS_MODG = WS_STG + 16384;
constexpr u32 WS_F2 = WS_MODG + 98304;
constexpr u32 WS_F3 = WS_F2 + 131072;
constexpr u32 WS_XG = 0x80000;
constexpr u32 WS_X1T = WS_XG + 4194304;
constexpr u32 WS_HH = WS_X1T + 2097152;
constexpr u32 WS_END = WS_HH + 7340032;
constexpr u32 WS_DBG = WS_END;
static_assert(WS_F3 + 65536 <= WS_XG, "ws map");

constexpr u32 L_V = 0;
constexpr u32 L_ST = 2048;
constexpr u32 L_STAT = 3840;
constexpr u32 L_GB = 4864;
constexpr u32 L_MISC = 5120;
constexpr u32 L_STAMP = 5376;
constexpr u32 L_X = 6144;
constexpr u32 L_FACT = L_X;
constexpr u32 L_KC = L_X;
constexpr u32 L_HR = L_KC + 33280;
constexpr u32 L_XB = L_HR + 9216;
constexpr u32 L_P = L_XB + 8192;
constexpr u32 L_TA = L_X;
constexpr u32 L_STG = L_X + 67072;
constexpr u32 SLOT = 14464, UPOFF = 7296;
constexpr u32 L_S5 = L_STG + 5 * SLOT;
constexpr u32 L_ROW = L_S5;
constexpr u32 L_MODP = L_ROW + 8320;
constexpr u32 L_G = L_MODP + 2048;
constexpr u32 L_DIAG = L_S5;
constexpr u32 L_TB = L_STG;
constexpr u32 L_END = 163840;
static_assert(L_G + 2704 <= L_STG + 6 * SLOT && L_STG + 6 * SLOT <= L_END, "lds map");
constexpr u32 M_CNT = L_MISC;
constexpr u32 M_RST = L_MISC + 32;
constexpr u32 M_INV = L_MISC + 64;

constexpr u32 kSpin = 40000;

struct Params {
  const void *x, *are, *aim, *bre, *bim, *cre, *cim, *logdt, *dskip, *wg, *wu, *wd, *c, *wmod;
  void *out, *ws;
};

DEV rsrc_t mk(const void* p, u32 bytes) { return __builtin_amdgcn_make_buffer_rsrc((void*)p, 0, bytes, 0x00020000); }
DEV float lo16(u32 v) { return __builtin_bit_cast(float, v << 16); }
DEV float hi16(u32 v) { return __builtin_bit_cast(float, v & 0xffff0000u); }
DEV u32 pk(float a, float b) { f2 t = {a, b}; return __builtin_bit_cast(u32, __builtin_convertvector(t, bf2)); }
DEV u32 pkh(float a, float b) { f2 t = {a, b}; return __builtin_bit_cast(u32, __builtin_convertvector(t, h2)); }
DEV float rbf(float a) { return lo16(pk(a, a)); }
DEV float fbits(u32 v) { return __builtin_bit_cast(float, v); }
DEV u32 ubits(float v) { return __builtin_bit_cast(u32, v); }
DEV float dot2(u32 a, u32 b, float c) {
  return __builtin_amdgcn_fdot2_f32_bf16(__builtin_bit_cast(bf2, a), __builtin_bit_cast(bf2, b), c, false);
}
template <int CTRL, int RM = 0xf> DEV float dppf(float v) {
  return fbits(__builtin_amdgcn_update_dpp(0u, ubits(v), CTRL, RM, 0xf, true));
}
template <int CTRL, int RM = 0xf> DEV u32 dppu(u32 v) { return __builtin_amdgcn_update_dpp(0u, v, CTRL, RM, 0xf, true); }
DEV float wave_sum(float v) {
  v += dppf<0xB1>(v);
  v += dppf<0x4E>(v);
  v += dppf<0x141>(v);
  v += dppf<0x140>(v);
  v += dppf<0x142, 0xa>(v);
  v += dppf<0x143, 0xc>(v);
  return fbits(__builtin_amdgcn_readlane(ubits(v), 63));
}
DEV u32 umax(u32 a, u32 b) { return a > b ? a : b; }
DEV u32 wave_umax(u32 v) {
  v = umax(v, dppu<0xB1>(v));
  v = umax(v, dppu<0x4E>(v));
  v = umax(v, dppu<0x141>(v));
  v = umax(v, dppu<0x140>(v));
  v = umax(v, dppu<0x142, 0xa>(v));
  v = umax(v, dppu<0x143, 0xc>(v));
  return __builtin_amdgcn_readlane(v, 63);
}

extern __shared__ __attribute__((aligned(16))) char smem[];
template <class T> DEV T lds_ld(u32 off) { return *(LAS T*)(size_t)(off); }
template <class T> DEV void lds_st(u32 off, T v) { *(LAS T*)(size_t)(off) = v; }
DEV v2u lds_tr(u32 off) {
  return __builtin_bit_cast(v2u, __builtin_amdgcn_ds_read_tr16_b64_v4i16((LAS s4v*)(size_t)(off)));
}
DEV v4u tr_frag(u32 a0, u32 a1) { v2u x = lds_tr(a0), y = lds_tr(a1); return v4u{x.x, x.y, y.x, y.y}; }
DEV f16v mfma32(v4u a, v4u b, f16v c) {
  return __builtin_amdgcn_mfma_f32_32x32x16_bf16(__builtin_bit_cast(bf8, a), __builtin_bit_cast(bf8, b), c, 0, 0, 0);
}
DEV f4 mfma16(v4u a, v4u b, f4 c) {
  return __builtin_amdgcn_mfma_f32_16x16x32_bf16(__builtin_bit_cast(bf8, a), __builtin_bit_cast(bf8, b), c, 0, 0, 0);
}
DEV f4 mfma16h(v4u a, v4u b, f4 c) {
  return __builtin_amdgcn_mfma_f32_16x16x32_f16(__builtin_bit_cast(h8, a), __builtin_bit_cast(h8, b), c, 0, 0, 0);
}

#define BAR() asm volatile("s_waitcnt lgkmcnt(0)\n\ts_barrier" ::: "memory")
#define VM0() asm volatile("s_waitcnt vmcnt(0)" ::: "memory")
#define LD128(dst, rs, vo, so, mods) \
  asm volatile("buffer_load_dwordx4 %0, %1, %2, %3 offen " mods : "=v"(dst) : "v"(vo), "s"(rs), "s"(so) : "memory")
#define LD64(dst, rs, vo, so, mods) \
  asm volatile("buffer_load_dwordx2 %0, %1, %2, %3 offen " mods : "=v"(dst) : "v"(vo), "s"(rs), "s"(so) : "memory")
#define LD16(dst, rs, vo, so, mods) \
  asm volatile("buffer_load_ushort %0, %1, %2, %3 offen " mods : "=v"(dst) : "v"(vo), "s"(rs), "s"(so) : "memory")
#define DMA(rs, vo, so, m, mods)                                                                          \
  asm volatile("s_mov_b32 m0, %3\n\ts_nop 0\n\tbuffer_load_dwordx4 %0, %1, %2 offen " mods " lds" ::"v"(vo), \
               "s"(rs), "s"(so), "s"(m)                                                                   \
               : "memory")
DEV void st128_dev(v4u v, rsrc_t rs, u32 vo, u32 so) {
  asm volatile("buffer_store_dwordx4 %0, %1, %2, %3 offen sc1\n\ts_nop 1" ::"v"(v), "v"(vo), "s"(rs), "s"(so) : "memory");
}
DEV void st64_dev(v2u v, rsrc_t rs, u32 vo, u32 so) { __builtin_amdgcn_raw_buffer_store_b64(v, rs, vo, so, 16); }
DEV bool tag_ok(v4u g, u32 ta, u32 tb) { return g.y == ta && g.w == tb; }

#define STAMP(i)

DEV f2 cmul(f2 a, f2 b) { return f2{a.x * b.x - a.y * b.y, a.x * b.y + a.y * b.x}; }
DEV f2 cexpv(float al, float be, float v) {
  const float e = __builtin_amdgcn_exp2f(al * v);
  const float r = __builtin_amdgcn_fractf(be * v);
  return f2{e * __builtin_amdgcn_cosf(r), e * __builtin_amdgcn_sinf(r)};
}
DEV u32 kidx(int lag) {
  const u32 bits = pk((float)lag, 0.f) & 0xffffu;
  const int id = lag < 256 ? lag : (int)bits - 0x4380 + 256;
  return (u32)(lag < 0 ? 6 : 8 + 2 * id);
}

DEV void stage_w(rsrc_t rg, rsrc_t ru, u32 vo, int unit, int slot, int ii, int gj) {
  const u32 so = unit * (32 * 3584) + gj * 224;
  const u32 m = L_STG + slot * SLOT + ii * 1024;
  DMA(rg, vo, so, m, "");
  DMA(ru, vo, so, m + UPOFF, "");
}
DEV u32 stage_vo(int ii, int l) { const u32 u = ii * 64 + l, row = u / 14; return row * 3584 + (u - row * 14) * 16; }

__global__ void __launch_bounds__(512, 2) fused_kernel(Params p) {
  const int tid = threadIdx.x, l = tid & 63;
  const int w = __builtin_amdgcn_readfirstlane(tid >> 6);
  const int b = blockIdx.x, xcd = b & 7, r = b >> 3;
  const int cp = xcd * 32 + r;
  const int gi = (xcd >> 1) * 4 + (r >> 3), gj = (xcd & 1) * 8 + (r & 7);
  const bool ismod = r < 24;
  const int mgrp = xcd * 6 + (r >> 2), mkq = r & 3;
  const rsrc_t rws = mk(p.ws, WS_END);
  u64 xt0; asm volatile("s_memrealtime %0\n\ts_waitcnt lgkmcnt(0)" : "=s"(xt0)::"memory");
  STAMP(0);

  v2u nonce; v4u row, wm0, wm1; u32 pAr, pAi, pBr, pBi, pCr, pCi, c0, c1;
  {
    const u32 zero = 0;
    LD64(nonce, rws, zero, zero, "sc1");
    const rsrc_t rx = mk(p.x, kL * kD * 2);
    LD128(row, rx, l * 16, (b * 8 + w) * 1024, "nt");
    const u32 vo = (((w >> 1) & 1) * 64 + l) * 2, so = (2 * cp + (w >> 2)) * 256;
    LD16(pAr, mk(p.are, kD * kN * 2), vo, so, "nt");
    LD16(pAi, mk(p.aim, kD * kN * 2), vo, so, "nt");
    LD16(pBr, mk(p.bre, kD * kN * 2), vo, so, "nt");
    LD16(pBi, mk(p.bim, kD * kN * 2), vo, so, "nt");
    LD16(pCr, mk(p.cre, kD * kN * 2), vo, so, "nt");
    LD16(pCi, mk(p.cim, kD * kN * 2), vo, so, "nt");
    if (ismod) {
      const rsrc_t rc = mk(p.c, kD * 2), rm = mk(p.wmod, kD * 6 * kD * 2);
      LD16(c0, rc, (tid >> 3) * 2, mkq * 256, "");
      LD16(c1, rc, (tid >> 3) * 2, mkq * 256 + 128, "");
      const u32 vm = (tid >> 3) * 6144 + (tid & 7) * 16, sm = mkq * 128 * 6144 + mgrp * 128;
      LD128(wm0, rm, vm, sm, "nt");
      LD128(wm1, rm, vm, sm + 64 * 6144, "nt");
    }
  }
  const u32 ldt2 = ((const CAS u32*)p.logdt)[cp], dsk2 = ((const CAS u32*)p.dskip)[cp];
  u32 kix[2][7];
#pragma unroll
  for (int k = 0; k < 2; k++)
#pragma unroll
    for (int e = 0; e < 7; e++) kix[k][e] = L_G + (tid >> 8) * 1352 + kidx(4 * ((tid & 255) + 256 * k) + e);
  const u32 svo0 = stage_vo(w < 7 ? w : 0, l), svo1 = stage_vo(w < 3 ? w + 4 : 0, l);
  if (tid < 4) lds_st<u32>(M_CNT + tid * 4, 0u);
  if (tid < 2) lds_st<v2u>(L_G + tid * 1352, v2u{0u, 0u});

#pragma unroll
  for (int k = 0; k < 2; k++)
    asm volatile("" : "+v"(kix[k][0]), "+v"(kix[k][1]), "+v"(kix[k][2]), "+v"(kix[k][3]), "+v"(kix[k][4]), "+v"(kix[k][5]), "+v"(kix[k][6]));
  if (ismod) asm volatile("s_waitcnt vmcnt(4)" ::: "memory");
  else asm volatile("s_waitcnt vmcnt(0)" ::: "memory");
  asm volatile("" : "+v"(nonce), "+v"(row), "+v"(pAr), "+v"(pAi), "+v"(pBr), "+v"(pBi), "+v"(pCr), "+v"(pCi)::"memory");
  STAMP(1);
  const u64 tag64 = (((u64)(u32)__builtin_amdgcn_readfirstlane(nonce.y) << 32) | (u64)(u32)__builtin_amdgcn_readfirstlane(nonce.x)) + 0x9E3779B97F4A7C15ull;
  const u32 tagA = (u32)tag64, tagB = (u32)(tag64 >> 32);
  {
    lds_st<v4u>(L_ROW + w * 1040 + l * 16, row);
    float s = dot2(row.x, 0x3f803f80u, 0.f), q = dot2(row.x, row.x, 0.f);
    s = dot2(row.y, 0x3f803f80u, s); q = dot2(row.y, row.y, q);
    s = dot2(row.z, 0x3f803f80u, s); q = dot2(row.z, row.z, q);
    s = dot2(row.w, 0x3f803f80u, s); q = dot2(row.w, row.w, q);
    const float mean = wave_sum(s) * (1.f / kD);
    const float var = rbf(fmaxf(wave_sum(q) * (1.f / kD) - mean * mean, 0.f));
    const float den = __builtin_amdgcn_sqrtf(rbf(var + 1e-5f));
    if (l == 0) lds_st<u32>(M_RST + w * 4, pk(mean, den));
  }
  BAR();
  {
    const u32 a = L_ROW + (tid & 3) * 2080 + (tid >> 2) * 4;
    const u32 d0 = lds_ld<u32>(a), d1 = lds_ld<u32>(a + 1040), d2 = lds_ld<u32>(a + 512), d3 = lds_ld<u32>(a + 512 + 1040);
    st128_dev(v4u{d0, tagA, d1, tagB}, rws, tid * 16, WS_XG + b * 16384);
    st128_dev(v4u{d2, tagA, d3, tagB}, rws, tid * 16, WS_XG + b * 16384 + 8192);
  }
  if (w == 7 && l < 4) {
    const v2u s2 = lds_ld<v2u>(M_RST + l * 8);
    st128_dev(v4u{s2.x, tagA, s2.y, tagB}, rws, l * 16, WS_STG + b * 64);
  }
  const rsrc_t rwg = mk(p.wg, kD * kF * 2), rwu = mk(p.wu, kD * kF * 2);
  if (w < 7) {
#pragma unroll
    for (int u = 0; u < 5; u++) stage_w(rwg, rwu, svo0, u, u, w, gj);
  }
  STAMP(2);
  {
    const int ch = w >> 2, mode = ((w >> 1) & 1) * 64 + l;
    const float dt = rbf(expf(ch ? hi16(ldt2) : lo16(ldt2)));
    const float al = (dt * lo16(pAr)) * 1.44269504088896341f, be = (dt * lo16(pAi)) * 0.159154943091895336f;
    const u32 fsw = ((mode >> 1) & 1) << 2 | ((mode >> 2) & 1) << 1 | (mode & 1);
    const u32 base = L_FACT + ch * 32768 + (w & 1) * 16384 + mode * 128;
    if ((w & 1) == 0) {
      const f2 e8 = cexpv(al, be, 8.f);
      f2 v[4][8];
      v[0][0] = f2{1.f, 0.f};
      v[1][0] = cexpv(al, be, 64.f); v[2][0] = cexpv(al, be, 128.f); v[3][0] = cexpv(al, be, 192.f);
#pragma unroll
      for (int i = 1; i < 8; i++)
#pragma unroll
        for (int c = 0; c < 4; c++) v[c][i] = cmul(v[c][i - 1], e8);
#pragma unroll
      for (int c = 0; c < 4; c++) {
        lds_st<v4u>(base + ((c ^ fsw) << 4), v4u{pkh(v[c][0].x, v[c][1].x), pkh(v[c][2].x, v[c][3].x), pkh(v[c][4].x, v[c][5].x), pkh(v[c][6].x, v[c][7].x)});
        lds_st<v4u>(base + (((4 + c) ^ fsw) << 4), v4u{pkh(v[c][0].y, v[c][1].y), pkh(v[c][2].y, v[c][3].y), pkh(v[c][4].y, v[c][5].y), pkh(v[c][6].y, v[c][7].y)});
      }
    } else {
      const f2 e1 = cexpv(al, be, 1.f), e256 = cexpv(al, be, 256.f);
      f2 wv = cmul(f2{lo16(pCr), lo16(pCi)}, f2{dt * lo16(pBr), dt * lo16(pBi)});
      u32 ex = wave_umax(umax(ubits(wv.x) & 0x7fffffffu, ubits(wv.y) & 0x7fffffffu)) >> 23;
      ex = ex < 14 ? 14 : (ex > 254 ? 254 : ex);
      wv *= fbits((267 - ex) << 23);
      if (l == 0) lds_st<u32>(M_INV + (w >> 1) * 4, (ex - 13) << 23);
      const f2 e2 = cmul(e1, e1), e4 = cmul(e2, e2);
      f2 v[24];
      v[0] = wv;
#pragma unroll
      for (int i = 1; i < 8; i++) v[i] = cmul(v[i - 1], e1);
      v[8] = cmul(wv, e256);
      v[12] = cmul(v[8], e256); v[14] = cmul(v[12], e256); v[16] = cmul(v[14], e256);
      v[9] = cmul(v[8], e2); v[10] = cmul(v[8], e4); v[11] = cmul(v[10], e2);
      v[13] = cmul(v[12], e4); v[15] = cmul(v[14], e4);
#pragma unroll
      for (int i = 17; i < 21; i++) v[i] = cmul(v[i - 1], e256);
      v[21] = v[22] = v[23] = f2{0.f, 0.f};
#pragma unroll
      for (int c = 0; c < 3; c++) {
        const f2* q = v + 8 * c;
        lds_st<v4u>(base + ((c ^ fsw) << 4), v4u{pkh(q[0].x, q[1].x), pkh(q[2].x, q[3].x), pkh(q[4].x, q[5].x), pkh(q[6].x, q[7].x)});
        lds_st<v4u>(base + (((4 + c) ^ fsw) << 4), v4u{pkh(-q[0].y, -q[1].y), pkh(-q[2].y, -q[3].y), pkh(-q[4].y, -q[5].y), pkh(-q[6].y, -q[7].y)});
      }
    }
  }
  STAMP(3);
  if (ismod) {
    if (w < 7) asm volatile("s_waitcnt vmcnt(12)" ::: "memory");
    else asm volatile("s_waitcnt vmcnt(3)" ::: "memory");
    asm volatile("" : "+v"(c0), "+v"(c1), "+v"(wm0), "+v"(wm1)::"memory");
    const float ca = lo16(c0), cb = lo16(c1);
    const float sa = rbf(ca / (1.f + expf(-ca))), sb = rbf(cb / (1.f + expf(-cb)));
    float a[8];
#pragma unroll
    for (int e = 0; e < 4; e++) {
      a[2 * e] = sa * lo16(wm0[e]) + sb * lo16(wm1[e]);
      a[2 * e + 1] = sa * hi16(wm0[e]) + sb * hi16(wm1[e]);
    }
    float t[4], u[2];
#pragma unroll
    for (int e = 0; e < 4; e++) {
      auto s = __builtin_amdgcn_permlane32_swap(ubits(a[e]), ubits(a[e + 4]), false, false);
      t[e] = fbits(s[0]) + fbits(s[1]);
    }
#pragma unroll
    for (int e = 0; e < 2; e++) {
      auto s = __builtin_amdgcn_permlane16_swap(ubits(t[e]), ubits(t[e + 2]), false, false);
      u[e] = fbits(s[0]) + fbits(s[1]);
      u[e] += dppf<0x128>(u[e]);
    }
    if ((l & 8) == 0) lds_st<f2>(L_MODP + w * 256 + ((l & 7) * 8 + (l >> 5) * 4 + ((l >> 4) & 1) * 2) * 4, f2{u[0], u[1]});
  }
  BAR();
  if (ismod && w == 7 && l < 32) {
    f2 s = lds_ld<f2>(L_MODP + l * 8);
#pragma unroll
    for (int k = 1; k < 8; k++) s += lds_ld<f2>(L_MODP + k * 256 + l * 8);
    st128_dev(v4u{ubits(s.x), tagA, ubits(s.y), tagB}, rws, l * 16, WS_MODG + mkq * 24576 + mgrp * 512);
  }
  {
    const int ch = w >> 2, mt = (w >> 1) & 1, nt = w & 1, q = l >> 4, rq = (l >> 2) & 3, pp = l & 3;
    f4 acc[2] = {f4{0, 0, 0, 0}, f4{0, 0, 0, 0}};
    u32 aa[2], ab[2];
#pragma unroll
    for (int rd = 0; rd < 2; rd++) {
      const u32 m = 4 * q + 2 * rd + (rq >> 1);
      const u32 fsw = ((m >> 1) & 1) << 2 | ((m >> 2) & 1) << 1 | (m & 1);
      const u32 base = L_FACT + ch * 32768 + m * 128 + (pp & 1) * 8;
      aa[rd] = base + 16384 + ((((rq & 1) * 4 + mt * 2 + (pp >> 1)) ^ fsw) << 4);
      ab[rd] = base + ((((rq & 1) * 4 + nt * 2 + (pp >> 1)) ^ fsw) << 4);
    }
#pragma unroll
    for (int ks = 0; ks < 8; ks++)
      acc[ks >> 2] = mfma16h(tr_frag(aa[0] + ks * 2048, aa[1] + ks * 2048), tr_frag(ab[0] + ks * 2048, ab[1] + ks * 2048), acc[ks >> 2]);
    const f2 inv = lds_ld<f2>(M_INV + ch * 8);
    const f4 kv = acc[0] * inv.x + acc[1] * inv.y;
    const u32 p01 = pk(kv.x, kv.y), p23 = pk(kv.z, kv.w), bp = 16 * nt + (l & 15), g = L_G + ch * 1352 + 8;
    if (mt == 0) {
      if (q < 3) lds_st<v2u>(g + (q < 2 ? 8 * bp + 4 * q : 256 + 4 * bp) * 2, v2u{p01, p23});
      else { lds_st<u32>(g + (384 + 2 * bp) * 2, p01); lds_st<u32>(g + (448 + 2 * bp) * 2, p23); }
    } else if (q == 0) {
      lds_st<u16>(g + (512 + bp) * 2, (u16)p01); lds_st<u16>(g + (544 + bp) * 2, (u16)(p01 >> 16));
      lds_st<u16>(g + (576 + bp) * 2, (u16)p23); lds_st<u16>(g + (608 + bp) * 2, (u16)(p23 >> 16));
    } else if (q == 1) lds_st<u16>(g + (640 + bp) * 2, (u16)p01);
  }
  BAR();
  STAMP(4);
  v4u gx0, gx1, gs0, gs1, gm;
  const u32 vgx = (tid >> 1) * 16384 + (tid & 1) * 32, lm = l < 12 ? l : 11;
  const u32 vgm = (lm & 3) * 24576 + (lm >> 2) * 4096;
  {
#pragma unroll
    for (int k = 0; k < 2; k++) {
      u32 v[7];
#pragma unroll
      for (int e = 0; e < 7; e++) v[e] = lds_ld<u16>(kix[k][e]);
      const u32 a = L_KC + (tid >> 8) * 16640 + ((tid & 255) + 256 * k + 4) * 8;
#pragma unroll
      for (int s = 0; s < 4; s++) lds_st<v2u>(a + s * 4160, v2u{v[s] | v[s + 1] << 16, v[s + 2] | v[s + 3] << 16});
    }
    if (tid < 8) {
      u32 v[7];
#pragma unroll
      for (int e = 0; e < 7; e++) v[e] = lds_ld<u16>(L_G + (tid >> 2) * 1352 + kidx(4 * (tid & 3) - 16 + e));
      const u32 a = L_KC + (tid >> 2) * 16640 + (tid & 3) * 8;
#pragma unroll
      for (int s = 0; s < 4; s++) lds_st<v2u>(a + s * 4160, v2u{v[s] | v[s + 1] << 16, v[s + 2] | v[s + 3] << 16});
    }
    if (tid < 64) lds_st<v4u>(L_HR + (tid >> 5) * 4608 + 4096 + (tid & 31) * 16, v4u{0u, 0u, 0u, 0u});
  }
  STAMP(5);
  for (u32 spin = 0;; spin++) {
    LD128(gx0, rws, vgx, WS_XG + cp * 64, "sc1");
    LD128(gx1, rws, vgx, WS_XG + cp * 64 + 16, "sc1");
    LD128(gs0, rws, tid * 32, WS_STG, "sc1");
    LD128(gs1, rws, tid * 32, WS_STG + 16, "sc1");
    LD128(gm, rws, vgm, WS_MODG + cp * 16, "sc1");
    asm volatile("s_waitcnt vmcnt(0)" : "+v"(gx0), "+v"(gx1), "+v"(gs0), "+v"(gs1), "+v"(gm)::"memory");
    const bool ok = tag_ok(gx0, tagA, tagB) && tag_ok(gx1, tagA, tagB) && tag_ok(gs0, tagA, tagB) && tag_ok(gs1, tagA, tagB) && tag_ok(gm, tagA, tagB);
    if (__all(ok) || spin > kSpin) break;
    __builtin_amdgcn_s_sleep(4);
  }
  STAMP(6);
  float ga0, ga1;
  {
    float m0 = fbits(gm.x), m1 = fbits(gm.z);
    m0 += dppf<0xB1>(m0); m1 += dppf<0xB1>(m1);
    m0 += dppf<0x4E>(m0); m1 += dppf<0x4E>(m1);
    const u32 mm = pk(m0, m1);
    const u32 msa = __builtin_amdgcn_readlane(mm, 0), mca = __builtin_amdgcn_readlane(mm, 4), mga = __builtin_amdgcn_readlane(mm, 8);
    const float sa0 = lo16(msa), sa1 = hi16(msa), s0 = rbf(1.f + lo16(mca)), s1 = rbf(1.f + hi16(mca));
    ga0 = lo16(mga); ga1 = hi16(mga);
    const u32 xs[4] = {gx0.x, gx0.z, gx1.x, gx1.z}, st[4] = {gs0.x, gs0.z, gs1.x, gs1.z};
    float h0[4], h1[4];
#pragma unroll
    for (int e = 0; e < 4; e++) {
      const float m = lo16(st[e]), rd = __builtin_amdgcn_rcpf(hi16(st[e]));
      const u32 xn = pk((lo16(xs[e]) - m) * rd, (hi16(xs[e]) - m) * rd);
      h0[e] = lo16(xn) * s0 + sa0; h1[e] = hi16(xn) * s1 + sa1;
    }
    lds_st<v2u>(L_HR + (2044 - 4 * tid) * 2, v2u{pk(h0[3], h0[2]), pk(h0[1], h0[0])});
    lds_st<v2u>(L_HR + 4608 + (2044 - 4 * tid) * 2, v2u{pk(h1[3], h1[2]), pk(h1[1], h1[0])});
    lds_st<v2u>(L_XB + tid * 8, v2u{(xs[0] & 0xffffu) | xs[1] << 16, (xs[2] & 0xffffu) | xs[3] << 16});
    lds_st<v2u>(L_XB + 4096 + tid * 8, v2u{xs[0] >> 16 | (xs[1] & 0xffff0000u), xs[2] >> 16 | (xs[3] & 0xffff0000u)});
  }
  BAR();
  STAMP(7);
  if (b == 0 && tid == 0) st64_dev(v2u{tagA, tagB}, rws, 0, WS_NONCE);

  v4u W[32];
  const u32 wtr = ((l >> 5) * 8 + ((l >> 2) & 3)) * 224 + ((l >> 1) & 1) * UPOFF + (w * 16 + ((l >> 4) & 1) * 8 + (l & 1) * 4) * 2 + L_STG;
#define READ_W(u0, n)                                                                            \
  _Pragma("unroll") for (int u = 0; u < (n); u++) _Pragma("unroll") for (int k = 0; k < 2; k++)  \
      W[2 * ((u0) + u) + k] = tr_frag(wtr + u * SLOT + k * 3584, wtr + u * SLOT + k * 3584 + 896)
  if (w < 7) { READ_W(0, 5); }
  const int t4 = tid & 255;
  const int ech = t4 >> 7;
  const float gav = ech ? ga1 : ga0, dsk = ech ? hi16(dsk2) : lo16(dsk2);
  auto conv_epilogue = [&](int half) {
    const int tile = (t4 >> 5) & 3, n = (t4 >> 1) & 15, qp = t4 & 1, t0 = 1024 * half + 256 * tile + 16 * n + 8 * qp;
    const u32 pa = L_P + ech * 4096 + tile * 1024 + (n + 32 * qp) * 16;
    const f4 y0 = lds_ld<f4>(pa) + lds_ld<f4>(pa + 8192), y1 = lds_ld<f4>(pa + 256) + lds_ld<f4>(pa + 8192 + 256);
    const v4u hv = lds_ld<v4u>(L_HR + ech * 4608 + (2040 - t0) * 2), xv = lds_ld<v4u>(L_XB + ech * 4096 + t0 * 2);
    const float y[8] = {y0.x, y0.y, y0.z, y0.w, y1.x, y1.y, y1.z, y1.w};
    v4u o;
#pragma unroll
    for (int e = 0; e < 4; e++) {
      const u32 yr = pk(y[2 * e], y[2 * e + 1]), hd = pk(hi16(hv[3 - e]) * dsk, lo16(hv[3 - e]) * dsk);
      const u32 y2 = pk(lo16(yr) + lo16(hd), hi16(yr) + hi16(hd));
      const u32 z = pk(gav * lo16(y2), gav * hi16(y2));
      o[e] = pk(lo16(xv[e]) + lo16(z), hi16(xv[e]) + hi16(z));
    }
    st128_dev(o, rws, (t0 >> 6) * 65536 + (t0 & 63) * 2 + ech * 128, WS_X1T + cp * 256);
  };
  auto publish_x1 = [&](int half) {
    u32 old = 0;
    if (l == 0) old = __hip_atomic_fetch_add((LAS u32*)(size_t)(M_CNT + half * 4), 1u, __ATOMIC_RELAXED, __HIP_MEMORY_SCOPE_WORKGROUP);
    if (__builtin_amdgcn_readfirstlane(old) == 3 && l < 8) st64_dev(v2u{tagA, tagB}, rws, l * 8192, WS_F2 + half * 65536 + b * 32);
  };
  if (w < 4) {
    const int ch = w & 1, bh = w >> 1, m = l & 15, q = l >> 4;
    const int lag0 = 16 * (q >> 1) + m - 8 * (q & 1) - 7, sg = (m + 1) & 3;
    const u32 ka = L_KC + ch * 16640 + sg * 4160 + (lag0 - sg + 16) * 2 + bh * 256;
    const u32 ha = L_HR + ch * 4608 + 4080 - 3584 - 32 * m + 32 * (q >> 1) - 16 * (q & 1) + bh * 256;
    auto ldA = [&](int a, int bb) { const v2u x = lds_ld<v2u>(ka + 512 * a + 64 * bb), y = lds_ld<v2u>(ka + 512 * a + 64 * bb + 8); return v4u{x.x, x.y, y.x, y.y}; };
    auto ldB = [&](int i, int bb) { return lds_ld<v4u>(ha + 512 * (7 - i) + 64 * bb); };
    f4 acc[4];
#pragma unroll
    for (int i = 0; i < 4; i++) acc[i] = f4{0, 0, 0, 0};
#pragma unroll
    for (int bb = 0; bb < 4; bb++) {
      v4u A[4], B[4];
#pragma unroll
      for (int i = 0; i < 4; i++) { A[i] = ldA(i, bb); B[i] = ldB(i, bb); }
#pragma unroll
      for (int a = 0; a < 4; a++)
#pragma unroll
        for (int i = a; i < 4; i++) acc[i] = mfma16(A[a], B[i - a], acc[i]);
    }
#pragma unroll
    for (int i = 0; i < 4; i++) lds_st<f4>(L_P + w * 4096 + i * 1024 + l * 16, acc[i]);
    BAR();
    STAMP(8);
#pragma unroll
    for (int u = 0; u < 6; u++) {
      stage_w(rwg, rwu, svo0, 5 + u, u, w, gj);
      if (w < 3) stage_w(rwg, rwu, svo1, 5 + u, u, w + 4, gj);
    }
#pragma unroll
    for (int i = 0; i < 4; i++) acc[i] = f4{0, 0, 0, 0};
#pragma unroll
    for (int bb = 0; bb < 4; bb++) {
      v4u B[8];
#pragma unroll
      for (int i = 0; i < 8; i++) B[i] = ldB(i, bb);
#pragma unroll
      for (int a = 0; a < 8; a++) {
        const v4u A = ldA(a, bb);
#pragma unroll
        for (int i = (a > 4 ? a : 4); i < 8; i++) acc[i - 4] = mfma16(A, B[i - a], acc[i - 4]);
      }
    }
    STAMP(9);
    BAR();
#pragma unroll
    for (int i = 0; i < 4; i++) lds_st<f4>(L_P + w * 4096 + i * 1024 + l * 16, acc[i]);
    VM0();
    BAR();
  } else {
    BAR();
    STAMP(8);
    conv_epilogue(0);
    VM0();
    publish_x1(0);
    v4u g[9];
    const u32 tg = t4 < 128 ? t4 : 127;
    for (u32 spin = 0;; spin++) {
#pragma unroll
      for (int k = 0; k < 4; k++) {
        LD128(g[k], rws, t4 * 16, WS_MODG + k * 24576 + 768 * 16, "sc1");
        LD128(g[4 + k], rws, t4 * 16, WS_MODG + k * 24576 + 1024 * 16, "sc1");
      }
      LD128(g[8], rws, (tg & 3) * 24576 + (tg >> 2) * 16, WS_MODG + (1280 + (gj & 7) * 32) * 16, "sc1");
      asm volatile("s_waitcnt vmcnt(0)" : "+v"(g[0]), "+v"(g[1]), "+v"(g[2]), "+v"(g[3]), "+v"(g[4]), "+v"(g[5]), "+v"(g[6]), "+v"(g[7]), "+v"(g[8])::"memory");
      bool ok = true;
#pragma unroll
      for (int k = 0; k < 9; k++) ok = ok && tag_ok(g[k], tagA, tagB);
      if (__all(ok) || spin > kSpin) break;
      __builtin_amdgcn_s_sleep(4);
    }
    const u32 sbp = pk((fbits(g[0].x) + fbits(g[1].x)) + (fbits(g[2].x) + fbits(g[3].x)), (fbits(g[0].z) + fbits(g[1].z)) + (fbits(g[2].z) + fbits(g[3].z)));
    const u32 cbp = pk((fbits(g[4].x) + fbits(g[5].x)) + (fbits(g[6].x) + fbits(g[7].x)), (fbits(g[4].z) + fbits(g[5].z)) + (fbits(g[6].z) + fbits(g[7].z)));
    lds_st<u32>(L_V + t4 * 4, pk(1.f + lo16(cbp), 1.f + hi16(cbp)));
    lds_st<u32>(L_V + 1024 + t4 * 4, sbp);
    float m0 = fbits(g[8].x), m1 = fbits(g[8].z);
    m0 += dppf<0xB1>(m0); m1 += dppf<0xB1>(m1);
    m0 += dppf<0x4E>(m0); m1 += dppf<0x4E>(m1);
    if (t4 < 128 && (t4 & 3) == 0) lds_st<f2>(L_GB + (t4 >> 2) * 8, f2{rbf(m0), rbf(m1)});
    STAMP(9);
    BAR();
    BAR();
    conv_epilogue(1);
  }
  STAMP(10);
  if (w < 7) { READ_W(5, 6); }
  BAR();
  if (w < 4) {
#pragma unroll
    for (int u = 0; u < 5; u++) {
      stage_w(rwg, rwu, svo0, 11 + u, u, w, gj);
      if (w < 3) stage_w(rwg, rwu, svo1, 11 + u, u, w + 4, gj);
    }
  }
  {
    const u32 sv = lds_ld<u16>(L_V + tid * 2), e = tid & 7, sh = sv << ((e & 1) * 16);
    lds_st<v4u>(L_DIAG + (tid >> 4) * 336 + (tid & 15) * 16, v4u{(e >> 1) == 0 ? sh : 0u, (e >> 1) == 1 ? sh : 0u, (e >> 1) == 2 ? sh : 0u, (e >> 1) == 3 ? sh : 0u});
    if (tid < 128) lds_st<v4u>(L_DIAG + (tid >> 2) * 336 + 256 + (tid & 3) * 16, lds_ld<v4u>(L_V + ((tid >> 1) & 1) * 1024 + (tid >> 2) * 32 + (tid & 1) * 16));
    if (tid < 32) lds_st<v4u>(L_DIAG + tid * 336 + 320, v4u{0u, 0u, 0u, 0u});
  }
  auto wait_x1 = [&](int half) {
    for (u32 spin = 0;; spin++) {
      v2u f0, f1, f2_, f3;
      const u32 so = WS_F2 + half * 65536 + xcd * 8192;
      LD64(f0, rws, l * 32, so, "sc1");
      LD64(f1, rws, l * 32, so + 2048, "sc1");
      LD64(f2_, rws, l * 32, so + 4096, "sc1");
      LD64(f3, rws, l * 32, so + 6144, "sc1");
      asm volatile("s_waitcnt vmcnt(0)" : "+v"(f0), "+v"(f1), "+v"(f2_), "+v"(f3)::"memory");
      const bool ok = f0.x == tagA && f0.y == tagB && f1.x == tagA && f1.y == tagB && f2_.x == tagA && f2_.y == tagB && f3.x == tagA && f3.y == tagB;
      if (__all(ok) || spin > kSpin) break;
      __builtin_amdgcn_s_sleep(4);
    }
  };
  if (w >= 4) { VM0(); publish_x1(1); }
  BAR();
  STAMP(11);
  f2 stv = {0.f, 0.f};
  const u32 dga = L_DIAG + ((l & 31) < 16 ? (((l >> 3) & 1) == (l >> 5) ? (l & 15) * 16 : 320) : ((l & 31) < 18 ? 256 + (l & 1) * 32 + (l >> 5) * 16 : 320));
#define PREP_W(s0, s1)                                                                       \
  _Pragma("unroll") for (int s = (s0); s < (s1); s++) {                                      \
    const f16v d = mfma32(lds_ld<v4u>(dga + s * 336), W[s], f16v{});                         \
    W[s] = v4u{pk(d[0], d[1]), pk(d[2], d[3]), pk(d[4], d[5]), pk(d[6], d[7])};              \
    stv += f2{d[8], d[9]};                                                                   \
  }
  const u32 tvo = (l >> 3) * 128 + (((l & 7) ^ (((l >> 4) & 1) << 2)) << 4);
  if (w < 7) { PREP_W(0, 22); VM0(); }
  else wait_x1(0);
  STAMP(12);
  BAR();
#pragma unroll
  for (int k = 0; k < 8; k++) DMA(rws, tvo, WS_X1T + gi * 65536 + (w * 8 + k) * 1024, L_TA + (w * 8 + k) * 1024, "sc1");
  if (w < 7) {
    READ_W(11, 5);
    PREP_W(22, 32);
    if (l < 32) {
      lds_st<float>(L_ST + w * 256 + l * 4, stv.x);
      lds_st<float>(L_ST + w * 256 + 128 + l * 4, (l & 8) ? stv.y : stv.y * -1.44269504088896341f);
    }
  }
  STAMP(13);
  VM0();
  BAR();
  STAMP(14);

  const u32 tra = ((l >> 5) * 4 + ((l >> 2) & 3)) * 128 + ((((l >> 4) & 1) * 32 + (l & 3) * 8) ^ (((l >> 3) & 1) << 6));
  const u32 rra = ((gj & 7) * 64 + (w >> 1) * 16 + (l >> 4) * 4 + ((l >> 2) & 3)) * 128 + (((gj >> 3) * 64 + (w & 1) * 32 + (l & 3) * 8) ^ (((l >> 3) & 1) << 6));
  const rsrc_t rwd = mk(p.wd, kF * kD * 2);
  const u32 dvo = (l >> 3) * 1024 + (((l & 7) ^ (((l >> 4) & 1) << 2)) << 4);
  const u32 d1base = w < 6 ? L_X + w * 10240 : 138752 + (w - 6) * 10240;
  f16v acc[2];
  auto gemm1 = [&](u32 tb, int s0, int s1) {
#pragma unroll
    for (int s = 0; s < 32; s++)
      if (s >= s0 && s < s1) {
        acc[0] = mfma32(W[s], tr_frag(tb + tra + s * 2048, tb + tra + s * 2048 + 1024), acc[0]);
        acc[1] = mfma32(W[s], tr_frag(tb + (tra ^ 64) + s * 2048, tb + (tra ^ 64) + s * 2048 + 1024), acc[1]);
      }
  };
  auto ln2_stats = [&](u32 tb, int half) {
#pragma unroll
    for (int nt = 0; nt < 2; nt++) {
      float s = 0.f, q = 0.f, s2 = 0.f, q2 = 0.f;
      const u32 a = tb + (tra ^ (nt * 64));
#pragma unroll 8
      for (int k = 0; k < 32; k++) {
        const v2u x = lds_tr(a + k * 2048), y = lds_tr(a + k * 2048 + 1024);
        s = dot2(x.x, 0x3f803f80u, s); q = dot2(x.x, x.x, q); s2 = dot2(x.y, 0x3f803f80u, s2); q2 = dot2(x.y, x.y, q2);
        s = dot2(y.x, 0x3f803f80u, s); q = dot2(y.x, y.x, q); s2 = dot2(y.y, 0x3f803f80u, s2); q2 = dot2(y.y, y.y, q2);
      }
      auto sw = __builtin_amdgcn_permlane32_swap(ubits(s + s2), ubits(q + q2), false, false);
      const float t = fbits(sw[0]) + fbits(sw[1]);
      auto s3 = __builtin_amdgcn_permlane32_swap(ubits(t), ubits(t), false, false);
      const float mean = fbits(s3[0]) * (1.f / kD);
      const float var = rbf(fmaxf(fbits(s3[1]) * (1.f / kD) - mean * mean, 0.f));
      const float den = rbf(__builtin_amdgcn_sqrtf(rbf(var + 1e-5f)));
      if (l < 32) lds_st<f2>(L_STAT + half * 512 + (nt * 32 + l) * 8, f2{rbf(mean), __builtin_amdgcn_rcpf(den)});
    }
  };
  auto glu_epilogue = [&](int half) {
    f4 S[4], T[4];
#pragma unroll
    for (int k = 0; k < 4; k++) {
      S[k] = lds_ld<f4>(L_ST + w * 256 + k * 32 + (l >> 5) * 16);
      T[k] = lds_ld<f4>(L_ST + w * 256 + 128 + k * 32 + (l >> 5) * 16);
    }
#pragma unroll
    for (int nt = 0; nt < 2; nt++) {
      const f2 st = lds_ld<f2>(L_STAT + half * 512 + (nt * 32 + (l & 31)) * 8);
      const float rg = st.y * -1.44269504088896341f;
      float o[8];
#pragma unroll
      for (int k = 0; k < 2; k++)
#pragma unroll
        for (int e = 0; e < 4; e++) {
          const float z = (acc[nt][8 * k + e] - st.x * S[2 * k][e]) * rg + T[2 * k][e];
          const float u = (acc[nt][8 * k + 4 + e] - st.x * S[2 * k + 1][e]) * st.y + T[2 * k + 1][e];
          o[4 * k + e] = u * __builtin_amdgcn_rcpf(1.f + __builtin_amdgcn_exp2f(z));
        }
      st128_dev(v4u{pk(o[0], o[1]), pk(o[2], o[3]), pk(o[4], o[5]), pk(o[6], o[7])}, rws, l * 16,
                WS_HH + ((half * 32 + 2 * gi + nt) * 112 + 7 * gj + w) * 1024);
    }
  };
  auto publish_hh = [&](int half) {
    u32 old = 0;
    if (l == 0) old = __hip_atomic_fetch_add((LAS u32*)(size_t)(M_CNT + 8 + half * 4), 1u, __ATOMIC_RELAXED, __HIP_MEMORY_SCOPE_WORKGROUP);
    if (__builtin_amdgcn_readfirstlane(old) == 6 && l == 0) st64_dev(v2u{tagA, tagB}, rws, 0, WS_F3 + ((half * 16 + gi) * 16 + gj) * 128);
  };
  auto stage_d = [&](int s0, int n, u32 base) {
#pragma unroll
    for (int k = 0; k < 2 * n; k++) DMA(rwd, dvo, (w * 224 + s0 * 16 + k * 8) * 1024 + (gj & 7) * 128, base + k * 1024, "");
  };
  const v2u resa = lds_tr(L_TA + rra);
  if (w < 7) {
    acc[0] = acc[1] = f16v{};
    gemm1(L_TA, 0, 32);
  } else {
    wait_x1(1);
#pragma unroll 8
    for (int k = 0; k < 64; k++) DMA(rws, tvo, WS_X1T + (16 + gi) * 65536 + k * 1024, L_TB + k * 1024, "sc1");
    ln2_stats(L_TA, 0);
    VM0();
  }
  STAMP(15);
  BAR();
  const v2u resb = lds_tr(L_TB + rra);
  if (w < 7) {
    glu_epilogue(0);
    acc[0] = acc[1] = f16v{};
    gemm1(L_TB, 0, 8);
    VM0();
    publish_hh(0);
    stage_d(0, 5, d1base);
    gemm1(L_TB, 8, 32);
  } else {
    stage_d(0, 5, d1base);
    ln2_stats(L_TB, 1);
  }
  STAMP(16);
  BAR();
  if (w < 7) glu_epilogue(1);
  VM0();
  if (w < 7) publish_hh(1);
  STAMP(17);

  v4u Wd[14][2];
#pragma unroll
  for (int s = 0; s < 5; s++)
#pragma unroll
    for (int mt = 0; mt < 2; mt++) Wd[s][mt] = tr_frag(d1base + (tra ^ (mt * 64)) + s * 2048, d1base + (tra ^ (mt * 64)) + s * 2048 + 1024);
  BAR();
  const u32 d2base = L_X + w * 18432;
  stage_d(5, 9, d2base);
  v4u hf[14];
  auto load_hh = [&](int half) {
    for (u32 spin = 0;; spin++) {
      v2u f;
      LD64(f, rws, (l & 1) * 128, WS_F3 + ((half * 16 + gi) * 16 + 2 * w) * 128, "sc1");
      asm volatile("s_waitcnt vmcnt(0)" : "+v"(f)::"memory");
      if (__all(f.x == tagA && f.y == tagB) || spin > kSpin) break;
      __builtin_amdgcn_s_sleep(4);
    }
#pragma unroll
    for (int s = 0; s < 14; s++)
      hf[s] = __builtin_amdgcn_raw_buffer_load_b128(rws, l * 16 + (s & 3) * 1024, WS_HH + ((half * 32 + 2 * gi + (gj >> 3)) * 112 + 14 * w + (s & ~3)) * 1024, 16);
  };
  auto gemm2 = [&](u32 red) {
    acc[0] = acc[1] = f16v{};
#pragma unroll
    for (int s = 0; s < 14; s++) {
      acc[0] = mfma32(Wd[s][0], hf[s], acc[0]);
      acc[1] = mfma32(Wd[s][1], hf[s], acc[1]);
    }
#pragma unroll
    for (int mt = 0; mt < 2; mt++)
#pragma unroll
      for (int g = 0; g < 4; g++)
        lds_st<f4>(red + w * 8192 + (mt * 4 + g) * 1024 + l * 16, f4{acc[mt][4 * g], acc[mt][4 * g + 1], acc[mt][4 * g + 2], acc[mt][4 * g + 3]});
  };
  const rsrc_t rout = mk(p.out, kL * kD * 2);
  auto out_epilogue = [&](int half, u32 red, v2u res) {
    const u32 a = red + ((w >> 1) * 4 + (l >> 4)) * 512 + ((w & 1) * 16 + (l & 15)) * 16;
    f4 s = lds_ld<f4>(a);
#pragma unroll
    for (int k = 1; k < 8; k++) s += lds_ld<f4>(a + k * 8192);
    const f4 gb = lds_ld<f4>(L_GB + ((w >> 1) * 4 + (l >> 4)) * 16);
    const u32 r01 = pk(s.x, s.y), r23 = pk(s.z, s.w);
    const u32 z01 = pk(gb.x * lo16(r01), gb.y * hi16(r01)), z23 = pk(gb.z * lo16(r23), gb.w * hi16(r23));
    const v2u o = {pk(lo16(res.x) + lo16(z01), hi16(res.x) + hi16(z01)), pk(lo16(res.y) + lo16(z23), hi16(res.y) + hi16(z23))};
    __builtin_amdgcn_raw_buffer_store_b64(o, rout, ((w & 1) * 16 + (l & 15)) * 1024 + ((w >> 1) * 4 + (l >> 4)) * 8,
                                          (half * 1024 + gi * 64 + (gj >> 3) * 32) * 1024 + (gj & 7) * 128, 0);
  };
  load_hh(0);
  STAMP(18);
#pragma unroll
  for (int s = 5; s < 14; s++)
#pragma unroll
    for (int mt = 0; mt < 2; mt++) Wd[s][mt] = tr_frag(d2base + (tra ^ (mt * 64)) + (s - 5) * 2048, d2base + (tra ^ (mt * 64)) + (s - 5) * 2048 + 1024);
  BAR();
  gemm2(L_X);
  STAMP(19);
  load_hh(1);
  STAMP(20);
  BAR();
  out_epilogue(0, L_X, resa);
  gemm2(L_STG);
  STAMP(21);
  BAR();
  out_epilogue(1, L_STG, resb);
  STAMP(22);
  { u64 xt1; asm volatile("s_memrealtime %0\n\ts_waitcnt lgkmcnt(0)" : "=s"(xt1)::"memory");
    if (l == 0) { u64* q = (u64*)((char*)p.ws + WS_DBG) + (b * 8 + w) * 2; q[0] = xt0; q[1] = xt1; } }
}

__global__ void k0(u64* q) { u64 t; asm volatile("s_memrealtime %0\n\ts_waitcnt lgkmcnt(0)" : "=s"(t)::"memory"); if (threadIdx.x == 0) q[8192] = t; }
__global__ void k2(u64* q) {
  u64 t; asm volatile("s_memrealtime %0\n\ts_waitcnt lgkmcnt(0)" : "=s"(t)::"memory");
  u64 mn = ~0ull, mx = 0;
  for (int i = 0; i < 2048; i++) { u64 a = q[2 * i], c = q[2 * i + 1]; mn = a < mn ? a : mn; mx = c > mx ? c : mx; }
  u64 d = 0 == 1 ? t - mx : (0 == 2 ? mn - q[8192] : (0 == 3 ? mx - mn : 0));
  if (d > 10000) d = 10000;
  u64 t0, t1; asm volatile("s_memrealtime %0\n\ts_waitcnt lgkmcnt(0)" : "=s"(t0)::"memory");
  do { asm volatile("s_memrealtime %0\n\ts_waitcnt lgkmcnt(0)" : "=s"(t1)::"memory"); } while (t1 - t0 < 10 * d);
}
}

extern "C" void kernel_launch(void* const* d_in, const int* in_sizes, int n_in, void* d_out, int out_size, void* d_ws,
                              size_t ws_size, hipStream_t stream) {
  using namespace s4glu;
  (void)in_sizes; (void)n_in; (void)out_size; (void)ws_size;
  Params p;
  p.x = d_in[0]; p.are = d_in[1]; p.aim = d_in[2]; p.bre = d_in[3]; p.bim = d_in[4]; p.cre = d_in[5]; p.cim = d_in[6];
  p.logdt = d_in[7]; p.dskip = d_in[8]; p.wg = d_in[10]; p.wu = d_in[11]; p.wd = d_in[12]; p.c = d_in[13]; p.wmod = d_in[14];
  p.out = d_out; p.ws = d_ws;
  (void)hipFuncSetAttribute((const void*)fused_kernel, hipFuncAttributeMaxDynamicSharedMemorySize, L_END);
  fused_kernel<<<256, 512, L_END, stream>>>(p);
  k2<<<1, 64, 0, stream>>>((u64*)((char*)d_ws + WS_DBG));
}
```

```cpp
#define AMPK 0
#include <hip/hip_runtime.h>
#include <stdint.h>

#define DBG 0

namespace s4glu {

typedef unsigned short u16;
typedef unsigned u32;
typedef unsigned long long u64;
typedef u32 v4u __attribute__((ext_vector_type(4)));
typedef u32 v2u __attribute__((ext_vector_type(2)));
typedef float f2 __attribute__((ext_vector_type(2)));
typedef float f4 __attribute__((ext_vector_type(4)));
typedef float f16v __attribute__((ext_vector_type(16)));
typedef __bf16 bf2 __attribute__((ext_vector_type(2)));
typedef __bf16 bf8 __attribute__((ext_vector_type(8)));
typedef _Float16 h2 __attribute__((ext_vector_type(2)));
typedef _Float16 h8 __attribute__((ext_vector_type(8)));
typedef short s4v __attribute__((ext_vector_type(4)));
typedef __amdgpu_buffer_rsrc_t rsrc_t;
#define LAS __attribute__((address_space(3)))
#define CAS __attribute__((address_space(4)))
#define DEV __device__ __forceinline__

constexpr int kL = 2048, kD = 512, kN = 128, kF = 1792;

constexpr u32 WS_NONCE = 0;
constexpr u32 WS_STG = 4096;
constexpr u32 WS_MODG = WS_STG + 16384;
constexpr u32 WS_F2 = WS_MODG + 98304;
constexpr u32 WS_F3 = WS_F2 + 131072;
constexpr u32 WS_XG = 0x80000;
constexpr u32 WS_X1T = WS_XG + 4194304;
constexpr u32 WS_HH = WS_X1T + 2097152;
constexpr u32 WS_END = WS_HH + 7340032;
constexpr u32 WS_DBG = WS_END;
static_assert(WS_F3 + 65536 <= WS_XG, "ws map");

constexpr u32 L_V = 0;
constexpr u32 L_ST = 2048;
constexpr u32 L_STAT = 3840;
constexpr u32 L_GB = 4864;
constexpr u32 L_MISC = 5120;
constexpr u32 L_STAMP = 5376;
constexpr u32 L_X = 6144;
constexpr u32 L_FACT = L_X;
constexpr u32 L_KC = L_X;
constexpr u32 L_HR = L_KC + 33280;
constexpr u32 L_XB = L_HR + 9216;
constexpr u32 L_P = L_XB + 8192;
constexpr u32 L_TA = L_X;
constexpr u32 L_STG = L_X + 67072;
constexpr u32 SLOT = 14464, UPOFF = 7296;
constexpr u32 L_S5 = L_STG + 5 * SLOT;
constexpr u32 L_ROW = L_S5;
constexpr u32 L_MODP = L_ROW + 8320;
constexpr u32 L_G = L_MODP + 2048;
constexpr u32 L_DIAG = L_S5;
constexpr u32 L_TB = L_STG;
constexpr u32 L_END = 163840;
static_assert(L_G + 2704 <= L_STG + 6 * SLOT && L_STG + 6 * SLOT <= L_END, "lds map");
constexpr u32 M_CNT = L_MISC;
constexpr u32 M_RST = L_MISC + 32;
constexpr u32 M_INV = L_MISC + 64;

constexpr u32 kSpin = 40000;

struct Params {
  const void *x, *are, *aim, *bre, *bim, *cre, *cim, *logdt, *dskip, *wg, *wu, *wd, *c, *wmod;
  void *out, *ws;
};

DEV rsrc_t mk(const void* p, u32 bytes) { return __builtin_amdgcn_make_buffer_rsrc((void*)p, 0, bytes, 0x00020000); }
DEV float lo16(u32 v) { return __builtin_bit_cast(float, v << 16); }
DEV float hi16(u32 v) { return __builtin_bit_cast(float, v & 0xffff0000u); }
DEV u32 pk(float a, float b) { f2 t = {a, b}; return __builtin_bit_cast(u32, __builtin_convertvector(t, bf2)); }
DEV u32 pkh(float a, float b) { f2 t = {a, b}; return __builtin_bit_cast(u32, __builtin_convertvector(t, h2)); }
DEV float rbf(float a) { return lo16(pk(a, a)); }
DEV float fbits(u32 v) { return __builtin_bit_cast(float, v); }
DEV u32 ubits(float v) { return __builtin_bit_cast(u32, v); }
DEV float dot2(u32 a, u32 b, float c) {
  return __builtin_amdgcn_fdot2_f32_bf16(__builtin_bit_cast(bf2, a), __builtin_bit_cast(bf2, b), c, false);
}
template <int CTRL, int RM = 0xf> DEV float dppf(float v) {
  return fbits(__builtin_amdgcn_update_dpp(0u, ubits(v), CTRL, RM, 0xf, true));
}
template <int CTRL, int RM = 0xf> DEV u32 dppu(u32 v) { return __builtin_amdgcn_update_dpp(0u, v, CTRL, RM, 0xf, true); }
DEV float wave_sum(float v) {
  v += dppf<0xB1>(v);
  v += dppf<0x4E>(v);
  v += dppf<0x141>(v);
  v += dppf<0x140>(v);
  v += dppf<0x142, 0xa>(v);
  v += dppf<0x143, 0xc>(v);
  return fbits(__builtin_amdgcn_readlane(ubits(v), 63));
}
DEV u32 umax(u32 a, u32 b) { return a > b ? a : b; }
DEV u32 wave_umax(u32 v) {
  v = umax(v, dppu<0xB1>(v));
  v = umax(v, dppu<0x4E>(v));
  v = umax(v, dppu<0x141>(v));
  v = umax(v, dppu<0x140>(v));
  v = umax(v, dppu<0x142, 0xa>(v));
  v = umax(v, dppu<0x143, 0xc>(v));
  return __builtin_amdgcn_readlane(v, 63);
}

extern __shared__ __attribute__((aligned(16))) char smem[];
template <class T> DEV T lds_ld(u32 off) { return *(LAS T*)(size_t)(off); }
template <class T> DEV void lds_st(u32 off, T v) { *(LAS T*)(size_t)(off) = v; }
DEV v2u lds_tr(u32 off) {
  return __builtin_bit_cast(v2u, __builtin_amdgcn_ds_read_tr16_b64_v4i16((LAS s4v*)(size_t)(off)));
}
DEV v4u tr_frag(u32 a0, u32 a1) { v2u x = lds_tr(a0), y = lds_tr(a1); return v4u{x.x, x.y, y.x, y.y}; }
DEV f16v mfma32(v4u a, v4u b, f16v c) {
  return __builtin_amdgcn_mfma_f32_32x32x16_bf16(__builtin_bit_cast(bf8, a), __builtin_bit_cast(bf8, b), c, 0, 0, 0);
}
DEV f4 mfma16(v4u a, v4u b, f4 c) {
  return __builtin_amdgcn_mfma_f32_16x16x32_bf16(__builtin_bit_cast(bf8, a), __builtin_bit_cast(bf8, b), c, 0, 0, 0);
}
DEV f4 mfma16h(v4u a, v4u b, f4 c) {
  return __builtin_amdgcn_mfma_f32_16x16x32_f16(__builtin_bit_cast(h8, a), __builtin_bit_cast(h8, b), c, 0, 0, 0);
}

#define BAR() asm volatile("s_waitcnt lgkmcnt(0)\n\ts_barrier" ::: "memory")
#define VM0() asm volatile("s_waitcnt vmcnt(0)" ::: "memory")
#define LD128(dst, rs, vo, so, mods) \
  asm volatile("buffer_load_dwordx4 %0, %1, %2, %3 offen " mods : "=v"(dst) : "v"(vo), "s"(rs), "s"(so) : "memory")
#define LD64(dst, rs, vo, so, mods) \
  asm volatile("buffer_load_dwordx2 %0, %1, %2, %3 offen " mods : "=v"(dst) : "v"(vo), "s"(rs), "s"(so) : "memory")
#define LD16(dst, rs, vo, so, mods) \
  asm volatile("buffer_load_ushort %0, %1, %2, %3 offen " mods : "=v"(dst) : "v"(vo), "s"(rs), "s"(so) : "memory")
#define DMA(rs, vo, so, m, mods)                                                                          \
  asm volatile("s_mov_b32 m0, %3\n\ts_nop 0\n\tbuffer_load_dwordx4 %0, %1, %2 offen " mods " lds" ::"v"(vo), \
               "s"(rs), "s"(so), "s"(m)                                                                   \
               : "memory")
DEV void st128_dev(v4u v, rsrc_t rs, u32 vo, u32 so) {
  asm volatile("buffer_store_dwordx4 %0, %1, %2, %3 offen sc1\n\ts_nop 1" ::"v"(v), "v"(vo), "s"(rs), "s"(so) : "memory");
}
DEV void st64_dev(v2u v, rsrc_t rs, u32 vo, u32 so) { __builtin_amdgcn_raw_buffer_store_b64(v, rs, vo, so, 16); }
DEV bool tag_ok(v4u g, u32 ta, u32 tb) { return g.y == ta && g.w == tb; }

#define STAMP(i)

DEV f2 cmul(f2 a, f2 b) { return f2{a.x * b.x - a.y * b.y, a.x * b.y + a.y * b.x}; }
DEV f2 cexpv(float al, float be, float v) {
  const float e = __builtin_amdgcn_exp2f(al * v);
  const float r = __builtin_amdgcn_fractf(be * v);
  return f2{e * __builtin_amdgcn_cosf(r), e * __builtin_amdgcn_sinf(r)};
}
DEV u32 kidx(int lag) {
  const u32 bits = pk((float)lag, 0.f) & 0xffffu;
  const int id = lag < 256 ? lag : (int)bits - 0x4380 + 256;
  return (u32)(lag < 0 ? 6 : 8 + 2 * id);
}

DEV void stage_w(rsrc_t rg, rsrc_t ru, u32 vo, int unit, int slot, int ii, int gj) {
  const u32 so = unit * (32 * 3584) + gj * 224;
  const u32 m = L_STG + slot * SLOT + ii * 1024;
  DMA(rg, vo, so, m, "");
  DMA(ru, vo, so, m + UPOFF, "");
}
DEV u32 stage_vo(int ii, int l) { const u32 u = ii * 64 + l, row = u / 14; return row * 3584 + (u - row * 14) * 16; }

__global__ void __launch_bounds__(512, 2) fused_kernel(Params p_) {
  u64 e0, e1;
  asm volatile("s_memrealtime %0\n\ts_waitcnt lgkmcnt(0)" : "=s"(e0)::"memory");
  const CAS Params* pp = (const CAS Params*)__builtin_amdgcn_kernarg_segment_ptr();
  asm volatile("" : "+s"(pp)::"memory");
  Params p = {pp->x, pp->are, pp->aim, pp->bre, pp->bim, pp->cre, pp->cim, pp->logdt, pp->dskip, pp->wg, pp->wu, pp->wd, pp->c, pp->wmod, pp->out, pp->ws};
  asm volatile("s_waitcnt lgkmcnt(0)\n\ts_memrealtime %0\n\ts_waitcnt lgkmcnt(0)" : "=s"(e1), "+s"(p.x), "+s"(p.ws)::"memory");
  const int tid = threadIdx.x, l = tid & 63;
  const int w = __builtin_amdgcn_readfirstlane(tid >> 6);
  const int b = blockIdx.x, xcd = b & 7, r = b >> 3;
  const int cp = xcd * 32 + r;
  const int gi = (xcd >> 1) * 4 + (r >> 3), gj = (xcd & 1) * 8 + (r & 7);
  const bool ismod = r < 24;
  const int mgrp = xcd * 6 + (r >> 2), mkq = r & 3;
  const rsrc_t rws = mk(p.ws, WS_END);
  STAMP(0);

  v2u nonce; v4u row, wm0, wm1; u32 pAr, pAi, pBr, pBi, pCr, pCi, c0, c1;
  {
    const u32 zero = 0;
    LD64(nonce, rws, zero, zero, "sc1");
    const rsrc_t rx = mk(p.x, kL * kD * 2);
    LD128(row, rx, l * 16, (b * 8 + w) * 1024, "nt");
    const u32 vo = (((w >> 1) & 1) * 64 + l) * 2, so = (2 * cp + (w >> 2)) * 256;
    LD16(pAr, mk(p.are, kD * kN * 2), vo, so, "nt");
    LD16(pAi, mk(p.aim, kD * kN * 2), vo, so, "nt");
    LD16(pBr, mk(p.bre, kD * kN * 2), vo, so, "nt");
    LD16(pBi, mk(p.bim, kD * kN * 2), vo, so, "nt");
    LD16(pCr, mk(p.cre, kD * kN * 2), vo, so, "nt");
    LD16(pCi, mk(p.cim, kD * kN * 2), vo, so, "nt");
    if (ismod) {
      const rsrc_t rc = mk(p.c, kD * 2), rm = mk(p.wmod, kD * 6 * kD * 2);
      LD16(c0, rc, (tid >> 3) * 2, mkq * 256, "");
      LD16(c1, rc, (tid >> 3) * 2, mkq * 256 + 128, "");
      const u32 vm = (tid >> 3) * 6144 + (tid & 7) * 16, sm = mkq * 128 * 6144 + mgrp * 128;
      LD128(wm0, rm, vm, sm, "nt");
      LD128(wm1, rm, vm, sm + 64 * 6144, "nt");
    }
  }
  const u32 ldt2 = ((const CAS u32*)p.logdt)[cp], dsk2 = ((const CAS u32*)p.dskip)[cp];
  u32 kix[2][7];
#pragma unroll
  for (int k = 0; k < 2; k++)
#pragma unroll
    for (int e = 0; e < 7; e++) kix[k][e] = L_G + (tid >> 8) * 1352 + kidx(4 * ((tid & 255) + 256 * k) + e);
  const u32 svo0 = stage_vo(w < 7 ? w : 0, l), svo1 = stage_vo(w < 3 ? w + 4 : 0, l);
  if (tid < 4) lds_st<u32>(M_CNT + tid * 4, 0u);
  if (tid < 2) lds_st<v2u>(L_G + tid * 1352, v2u{0u, 0u});

#pragma unroll
  for (int k = 0; k < 2; k++)
    asm volatile("" : "+v"(kix[k][0]), "+v"(kix[k][1]), "+v"(kix[k][2]), "+v"(kix[k][3]), "+v"(kix[k][4]), "+v"(kix[k][5]), "+v"(kix[k][6]));
  if (ismod) asm volatile("s_waitcnt vmcnt(4)" ::: "memory");
  else asm volatile("s_waitcnt vmcnt(0)" ::: "memory");
  asm volatile("" : "+v"(nonce), "+v"(row), "+v"(pAr), "+v"(pAi), "+v"(pBr), "+v"(pBi), "+v"(pCr), "+v"(pCi)::"memory");
  STAMP(1);
  const u64 tag64 = (((u64)(u32)__builtin_amdgcn_readfirstlane(nonce.y) << 32) | (u64)(u32)__builtin_amdgcn_readfirstlane(nonce.x)) + 0x9E3779B97F4A7C15ull;
  const u32 tagA = (u32)tag64, tagB = (u32)(tag64 >> 32);
  {
    lds_st<v4u>(L_ROW + w * 1040 + l * 16, row);
    float s = dot2(row.x, 0x3f803f80u, 0.f), q = dot2(row.x, row.x, 0.f);
    s = dot2(row.y, 0x3f803f80u, s); q = dot2(row.y, row.y, q);
    s = dot2(row.z, 0x3f803f80u, s); q = dot2(row.z, row.z, q);
    s = dot2(row.w, 0x3f803f80u, s); q = dot2(row.w, row.w, q);
    const float mean = wave_sum(s) * (1.f / kD);
    const float var = rbf(fmaxf(wave_sum(q) * (1.f / kD) - mean * mean, 0.f));
    const float den = __builtin_amdgcn_sqrtf(rbf(var + 1e-5f));
    if (l == 0) lds_st<u32>(M_RST + w * 4, pk(mean, den));
  }
  BAR();
  {
    const u32 a = L_ROW + (tid & 3) * 2080 + (tid >> 2) * 4;
    const u32 d0 = lds_ld<u32>(a), d1 = lds_ld<u32>(a + 1040), d2 = lds_ld<u32>(a + 512), d3 = lds_ld<u32>(a + 512 + 1040);
    st128_dev(v4u{d0, tagA, d1, tagB}, rws, tid * 16, WS_XG + b * 16384);
    st128_dev(v4u{d2, tagA, d3, tagB}, rws, tid * 16, WS_XG + b * 16384 + 8192);
  }
  if (w == 7 && l < 4) {
    const v2u s2 = lds_ld<v2u>(M_RST + l * 8);
    st128_dev(v4u{s2.x, tagA, s2.y, tagB}, rws, l * 16, WS_STG + b * 64);
  }
  const rsrc_t rwg = mk(p.wg, kD * kF * 2), rwu = mk(p.wu, kD * kF * 2);
  if (w < 7) {
#pragma unroll
    for (int u = 0; u < 5; u++) stage_w(rwg, rwu, svo0, u, u, w, gj);
  }
  STAMP(2);
  {
    const int ch = w >> 2, mode = ((w >> 1) & 1) * 64 + l;
    const float dt = rbf(expf(ch ? hi16(ldt2) : lo16(ldt2)));
    const float al = (dt * lo16(pAr)) * 1.44269504088896341f, be = (dt * lo16(pAi)) * 0.159154943091895336f;
    const u32 fsw = ((mode >> 1) & 1) << 2 | ((mode >> 2) & 1) << 1 | (mode & 1);
    const u32 base = L_FACT + ch * 32768 + (w & 1) * 16384 + mode * 128;
    if ((w & 1) == 0) {
      const f2 e8 = cexpv(al, be, 8.f);
      f2 v[4][8];
      v[0][0] = f2{1.f, 0.f};
      v[1][0] = cexpv(al, be, 64.f); v[2][0] = cexpv(al, be, 128.f); v[3][0] = cexpv(al, be, 192.f);
#pragma unroll
      for (int i = 1; i < 8; i++)
#pragma unroll
        for (int c = 0; c < 4; c++) v[c][i] = cmul(v[c][i - 1], e8);
#pragma unroll
      for (int c = 0; c < 4; c++) {
        lds_st<v4u>(base + ((c ^ fsw) << 4), v4u{pkh(v[c][0].x, v[c][1].x), pkh(v[c][2].x, v[c][3].x), pkh(v[c][4].x, v[c][5].x), pkh(v[c][6].x, v[c][7].x)});
        lds_st<v4u>(base + (((4 + c) ^ fsw) << 4), v4u{pkh(v[c][0].y, v[c][1].y), pkh(v[c][2].y, v[c][3].y), pkh(v[c][4].y, v[c][5].y), pkh(v[c][6].y, v[c][7].y)});
      }
    } else {
      const f2 e1 = cexpv(al, be, 1.f), e256 = cexpv(al, be, 256.f);
      f2 wv = cmul(f2{lo16(pCr), lo16(pCi)}, f2{dt * lo16(pBr), dt * lo16(pBi)});
      u32 ex = wave_umax(umax(ubits(wv.x) & 0x7fffffffu, ubits(wv.y) & 0x7fffffffu)) >> 23;
      ex = ex < 14 ? 14 : (ex > 254 ? 254 : ex);
      wv *= fbits((267 - ex) << 23);
      if (l == 0) lds_st<u32>(M_INV + (w >> 1) * 4, (ex - 13) << 23);
      const f2 e2 = cmul(e1, e1), e4 = cmul(e2, e2);
      f2 v[24];
      v[0] = wv;
#pragma unroll
      for (int i = 1; i < 8; i++) v[i] = cmul(v[i - 1], e1);
      v[8] = cmul(wv, e256);
      v[12] = cmul(v[8], e256); v[14] = cmul(v[12], e256); v[16] = cmul(v[14], e256);
      v[9] = cmul(v[8], e2); v[10] = cmul(v[8], e4); v[11] = cmul(v[10], e2);
      v[13] = cmul(v[12], e4); v[15] = cmul(v[14], e4);
#pragma unroll
      for (int i = 17; i < 21; i++) v[i] = cmul(v[i - 1], e256);
      v[21] = v[22] = v[23] = f2{0.f, 0.f};
#pragma unroll
      for (int c = 0; c < 3; c++) {
        const f2* q = v + 8 * c;
        lds_st<v4u>(base + ((c ^ fsw) << 4), v4u{pkh(q[0].x, q[1].x), pkh(q[2].x, q[3].x), pkh(q[4].x, q[5].x), pkh(q[6].x, q[7].x)});
        lds_st<v4u>(base + (((4 + c) ^ fsw) << 4), v4u{pkh(-q[0].y, -q[1].y), pkh(-q[2].y, -q[3].y), pkh(-q[4].y, -q[5].y), pkh(-q[6].y, -q[7].y)});
      }
    }
  }
  STAMP(3);
  if (ismod) {
    if (w < 7) asm volatile("s_waitcnt vmcnt(12)" ::: "memory");
    else asm volatile("s_waitcnt vmcnt(3)" ::: "memory");
    asm volatile("" : "+v"(c0), "+v"(c1), "+v"(wm0), "+v"(wm1)::"memory");
    const float ca = lo16(c0), cb = lo16(c1);
    const float sa = rbf(ca / (1.f + expf(-ca))), sb = rbf(cb / (1.f + expf(-cb)));
    float a[8];
#pragma unroll
    for (int e = 0; e < 4; e++) {
      a[2 * e] = sa * lo16(wm0[e]) + sb * lo16(wm1[e]);
      a[2 * e + 1] = sa * hi16(wm0[e]) + sb * hi16(wm1[e]);
    }
    float t[4], u[2];
#pragma unroll
    for (int e = 0; e < 4; e++) {
      auto s = __builtin_amdgcn_permlane32_swap(ubits(a[e]), ubits(a[e + 4]), false, false);
      t[e] = fbits(s[0]) + fbits(s[1]);
    }
#pragma unroll
    for (int e = 0; e < 2; e++) {
      auto s = __builtin_amdgcn_permlane16_swap(ubits(t[e]), ubits(t[e + 2]), false, false);
      u[e] = fbits(s[0]) + fbits(s[1]);
      u[e] += dppf<0x128>(u[e]);
    }
    if ((l & 8) == 0) lds_st<f2>(L_MODP + w * 256 + ((l & 7) * 8 + (l >> 5) * 4 + ((l >> 4) & 1) * 2) * 4, f2{u[0], u[1]});
  }
  BAR();
  if (ismod && w == 7 && l < 32) {
    f2 s = lds_ld<f2>(L_MODP + l * 8);
#pragma unroll
    for (int k = 1; k < 8; k++) s += lds_ld<f2>(L_MODP + k * 256 + l * 8);
    st128_dev(v4u{ubits(s.x), tagA, ubits(s.y), tagB}, rws, l * 16, WS_MODG + mkq * 24576 + mgrp * 512);
  }
  {
    const int ch = w >> 2, mt = (w >> 1) & 1, nt = w & 1, q = l >> 4, rq = (l >> 2) & 3, pp = l & 3;
    f4 acc[2] = {f4{0, 0, 0, 0}, f4{0, 0, 0, 0}};
    u32 aa[2], ab[2];
#pragma unroll
    for (int rd = 0; rd < 2; rd++) {
      const u32 m = 4 * q + 2 * rd + (rq >> 1);
      const u32 fsw = ((m >> 1) & 1) << 2 | ((m >> 2) & 1) << 1 | (m & 1);
      const u32 base = L_FACT + ch * 32768 + m * 128 + (pp & 1) * 8;
      aa[rd] = base + 16384 + ((((rq & 1) * 4 + mt * 2 + (pp >> 1)) ^ fsw) << 4);
      ab[rd] = base + ((((rq & 1) * 4 + nt * 2 + (pp >> 1)) ^ fsw) << 4);
    }
#pragma unroll
    for (int ks = 0; ks < 8; ks++)
      acc[ks >> 2] = mfma16h(tr_frag(aa[0] + ks * 2048, aa[1] + ks * 2048), tr_frag(ab[0] + ks * 2048, ab[1] + ks * 2048), acc[ks >> 2]);
    const f2 inv = lds_ld<f2>(M_INV + ch * 8);
    const f4 kv = acc[0] * inv.x + acc[1] * inv.y;
    const u32 p01 = pk(kv.x, kv.y), p23 = pk(kv.z, kv.w), bp = 16 * nt + (l & 15), g = L_G + ch * 1352 + 8;
    if (mt == 0) {
      if (q < 3) lds_st<v2u>(g + (q < 2 ? 8 * bp + 4 * q : 256 + 4 * bp) * 2, v2u{p01, p23});
      else { lds_st<u32>(g + (384 + 2 * bp) * 2, p01); lds_st<u32>(g + (448 + 2 * bp) * 2, p23); }
    } else if (q == 0) {
      lds_st<u16>(g + (512 + bp) * 2, (u16)p01); lds_st<u16>(g + (544 + bp) * 2, (u16)(p01 >> 16));
      lds_st<u16>(g + (576 + bp) * 2, (u16)p23); lds_st<u16>(g + (608 + bp) * 2, (u16)(p23 >> 16));
    } else if (q == 1) lds_st<u16>(g + (640 + bp) * 2, (u16)p01);
  }
  BAR();
  STAMP(4);
  v4u gx0, gx1, gs0, gs1, gm;
  const u32 vgx = (tid >> 1) * 16384 + (tid & 1) * 32, lm = l < 12 ? l : 11;
  const u32 vgm = (lm & 3) * 24576 + (lm >> 2) * 4096;
  {
#pragma unroll
    for (int k = 0; k < 2; k++) {
      u32 v[7];
#pragma unroll
      for (int e = 0; e < 7; e++) v[e] = lds_ld<u16>(kix[k][e]);
      const u32 a = L_KC + (tid >> 8) * 16640 + ((tid & 255) + 256 * k + 4) * 8;
#pragma unroll
      for (int s = 0; s < 4; s++) lds_st<v2u>(a + s * 4160, v2u{v[s] | v[s + 1] << 16, v[s + 2] | v[s + 3] << 16});
    }
    if (tid < 8) {
      u32 v[7];
#pragma unroll
      for (int e = 0; e < 7; e++) v[e] = lds_ld<u16>(L_G + (tid >> 2) * 1352 + kidx(4 * (tid & 3) - 16 + e));
      const u32 a = L_KC + (tid >> 2) * 16640 + (tid & 3) * 8;
#pragma unroll
      for (int s = 0; s < 4; s++) lds_st<v2u>(a + s * 4160, v2u{v[s] | v[s + 1] << 16, v[s + 2] | v[s + 3] << 16});
    }
    if (tid < 64) lds_st<v4u>(L_HR + (tid >> 5) * 4608 + 4096 + (tid & 31) * 16, v4u{0u, 0u, 0u, 0u});
  }
  STAMP(5);
  for (u32 spin = 0;; spin++) {
    LD128(gx0, rws, vgx, WS_XG + cp * 64, "sc1");
    LD128(gx1, rws, vgx, WS_XG + cp * 64 + 16, "sc1");
    LD128(gs0, rws, tid * 32, WS_STG, "sc1");
    LD128(gs1, rws, tid * 32, WS_STG + 16, "sc1");
    LD128(gm, rws, vgm, WS_MODG + cp * 16, "sc1");
    asm volatile("s_waitcnt vmcnt(0)" : "+v"(gx0), "+v"(gx1), "+v"(gs0), "+v"(gs1), "+v"(gm)::"memory");
    const bool ok = tag_ok(gx0, tagA, tagB) && tag_ok(gx1, tagA, tagB) && tag_ok(gs0, tagA, tagB) && tag_ok(gs1, tagA, tagB) && tag_ok(gm, tagA, tagB);
    if (__all(ok) || spin > kSpin) break;
    __builtin_amdgcn_s_sleep(4);
  }
  STAMP(6);
  float ga0, ga1;
  {
    float m0 = fbits(gm.x), m1 = fbits(gm.z);
    m0 += dppf<0xB1>(m0); m1 += dppf<0xB1>(m1);
    m0 += dppf<0x4E>(m0); m1 += dppf<0x4E>(m1);
    const u32 mm = pk(m0, m1);
    const u32 msa = __builtin_amdgcn_readlane(mm, 0), mca = __builtin_amdgcn_readlane(mm, 4), mga = __builtin_amdgcn_readlane(mm, 8);
    const float sa0 = lo16(msa), sa1 = hi16(msa), s0 = rbf(1.f + lo16(mca)), s1 = rbf(1.f + hi16(mca));
    ga0 = lo16(mga); ga1 = hi16(mga);
    const u32 xs[4] = {gx0.x, gx0.z, gx1.x, gx1.z}, st[4] = {gs0.x, gs0.z, gs1.x, gs1.z};
    float h0[4], h1[4];
#pragma unroll
    for (int e = 0; e < 4; e++) {
      const float m = lo16(st[e]), rd = __builtin_amdgcn_rcpf(hi16(st[e]));
      const u32 xn = pk((lo16(xs[e]) - m) * rd, (hi16(xs[e]) - m) * rd);
      h0[e] = lo16(xn) * s0 + sa0; h1[e] = hi16(xn) * s1 + sa1;
    }
    lds_st<v2u>(L_HR + (2044 - 4 * tid) * 2, v2u{pk(h0[3], h0[2]), pk(h0[1], h0[0])});
    lds_st<v2u>(L_HR + 4608 + (2044 - 4 * tid) * 2, v2u{pk(h1[3], h1[2]), pk(h1[1], h1[0])});
    lds_st<v2u>(L_XB + tid * 8, v2u{(xs[0] & 0xffffu) | xs[1] << 16, (xs[2] & 0xffffu) | xs[3] << 16});
    lds_st<v2u>(L_XB + 4096 + tid * 8, v2u{xs[0] >> 16 | (xs[1] & 0xffff0000u), xs[2] >> 16 | (xs[3] & 0xffff0000u)});
  }
  BAR();
  STAMP(7);
  if (b == 0 && tid == 0) st64_dev(v2u{tagA, tagB}, rws, 0, WS_NONCE);

  v4u W[32];
  const u32 wtr = ((l >> 5) * 8 + ((l >> 2) & 3)) * 224 + ((l >> 1) & 1) * UPOFF + (w * 16 + ((l >> 4) & 1) * 8 + (l & 1) * 4) * 2 + L_STG;
#define READ_W(u0, n)                                                                            \
  _Pragma("unroll") for (int u = 0; u < (n); u++) _Pragma("unroll") for (int k = 0; k < 2; k++)  \
      W[2 * ((u0) + u) + k] = tr_frag(wtr + u * SLOT + k * 3584, wtr + u * SLOT + k * 3584 + 896)
  if (w < 7) { READ_W(0, 5); }
  const int t4 = tid & 255;
  const int ech = t4 >> 7;
  const float gav = ech ? ga1 : ga0, dsk = ech ? hi16(dsk2) : lo16(dsk2);
  auto conv_epilogue = [&](int half) {
    const int tile = (t4 >> 5) & 3, n = (t4 >> 1) & 15, qp = t4 & 1, t0 = 1024 * half + 256 * tile + 16 * n + 8 * qp;
    const u32 pa = L_P + ech * 4096 + tile * 1024 + (n + 32 * qp) * 16;
    const f4 y0 = lds_ld<f4>(pa) + lds_ld<f4>(pa + 8192), y1 = lds_ld<f4>(pa + 256) + lds_ld<f4>(pa + 8192 + 256);
    const v4u hv = lds_ld<v4u>(L_HR + ech * 4608 + (2040 - t0) * 2), xv = lds_ld<v4u>(L_XB + ech * 4096 + t0 * 2);
    const float y[8] = {y0.x, y0.y, y0.z, y0.w, y1.x, y1.y, y1.z, y1.w};
    v4u o;
#pragma unroll
    for (int e = 0; e < 4; e++) {
      const u32 yr = pk(y[2 * e], y[2 * e + 1]), hd = pk(hi16(hv[3 - e]) * dsk, lo16(hv[3 - e]) * dsk);
      const u32 y2 = pk(lo16(yr) + lo16(hd), hi16(yr) + hi16(hd));
      const u32 z = pk(gav * lo16(y2), gav * hi16(y2));
      o[e] = pk(lo16(xv[e]) + lo16(z), hi16(xv[e]) + hi16(z));
    }
    st128_dev(o, rws, (t0 >> 6) * 65536 + (t0 & 63) * 2 + ech * 128, WS_X1T + cp * 256);
  };
  auto publish_x1 = [&](int half) {
    u32 old = 0;
    if (l == 0) old = __hip_atomic_fetch_add((LAS u32*)(size_t)(M_CNT + half * 4), 1u, __ATOMIC_RELAXED, __HIP_MEMORY_SCOPE_WORKGROUP);
    if (__builtin_amdgcn_readfirstlane(old) == 3 && l < 8) st64_dev(v2u{tagA, tagB}, rws, l * 8192, WS_F2 + half * 65536 + b * 32);
  };
  if (w < 4) {
    const int ch = w & 1, bh = w >> 1, m = l & 15, q = l >> 4;
    const int lag0 = 16 * (q >> 1) + m - 8 * (q & 1) - 7, sg = (m + 1) & 3;
    const u32 ka = L_KC + ch * 16640 + sg * 4160 + (lag0 - sg + 16) * 2 + bh * 256;
    const u32 ha = L_HR + ch * 4608 + 4080 - 3584 - 32 * m + 32 * (q >> 1) - 16 * (q & 1) + bh * 256;
    auto ldA = [&](int a, int bb) { const v2u x = lds_ld<v2u>(ka + 512 * a + 64 * bb), y = lds_ld<v2u>(ka + 512 * a + 64 * bb + 8); return v4u{x.x, x.y, y.x, y.y}; };
    auto ldB = [&](int i, int bb) { return lds_ld<v4u>(ha + 512 * (7 - i) + 64 * bb); };
    f4 acc[4];
#pragma unroll
    for (int i = 0; i < 4; i++) acc[i] = f4{0, 0, 0, 0};
#pragma unroll
    for (int bb = 0; bb < 4; bb++) {
      v4u A[4], B[4];
#pragma unroll
      for (int i = 0; i < 4; i++) { A[i] = ldA(i, bb); B[i] = ldB(i, bb); }
#pragma unroll
      for (int a = 0; a < 4; a++)
#pragma unroll
        for (int i = a; i < 4; i++) acc[i] = mfma16(A[a], B[i - a], acc[i]);
    }
#pragma unroll
    for (int i = 0; i < 4; i++) lds_st<f4>(L_P + w * 4096 + i * 1024 + l * 16, acc[i]);
    BAR();
    STAMP(8);
#pragma unroll
    for (int u = 0; u < 6; u++) {
      stage_w(rwg, rwu, svo0, 5 + u, u, w, gj);
      if (w < 3) stage_w(rwg, rwu, svo1, 5 + u, u, w + 4, gj);
    }
#pragma unroll
    for (int i = 0; i < 4; i++) acc[i] = f4{0, 0, 0, 0};
#pragma unroll
    for (int bb = 0; bb < 4; bb++) {
      v4u B[8];
#pragma unroll
      for (int i = 0; i < 8; i++) B[i] = ldB(i, bb);
#pragma unroll
      for (int a = 0; a < 8; a++) {
        const v4u A = ldA(a, bb);
#pragma unroll
        for (int i = (a > 4 ? a : 4); i < 8; i++) acc[i - 4] = mfma16(A, B[i - a], acc[i - 4]);
      }
    }
    STAMP(9);
    BAR();
#pragma unroll
    for (int i = 0; i < 4; i++) lds_st<f4>(L_P + w * 4096 + i * 1024 + l * 16, acc[i]);
    VM0();
    BAR();
  } else {
    BAR();
    STAMP(8);
    conv_epilogue(0);
    VM0();
    publish_x1(0);
    v4u g[9];
    const u32 tg = t4 < 128 ? t4 : 127;
    for (u32 spin = 0;; spin++) {
#pragma unroll
      for (int k = 0; k < 4; k++) {
        LD128(g[k], rws, t4 * 16, WS_MODG + k * 24576 + 768 * 16, "sc1");
        LD128(g[4 + k], rws, t4 * 16, WS_MODG + k * 24576 + 1024 * 16, "sc1");
      }
      LD128(g[8], rws, (tg & 3) * 24576 + (tg >> 2) * 16, WS_MODG + (1280 + (gj & 7) * 32) * 16, "sc1");
      asm volatile("s_waitcnt vmcnt(0)" : "+v"(g[0]), "+v"(g[1]), "+v"(g[2]), "+v"(g[3]), "+v"(g[4]), "+v"(g[5]), "+v"(g[6]), "+v"(g[7]), "+v"(g[8])::"memory");
      bool ok = true;
#pragma unroll
      for (int k = 0; k < 9; k++) ok = ok && tag_ok(g[k], tagA, tagB);
      if (__all(ok) || spin > kSpin) break;
      __builtin_amdgcn_s_sleep(4);
    }
    const u32 sbp = pk((fbits(g[0].x) + fbits(g[1].x)) + (fbits(g[2].x) + fbits(g[3].x)), (fbits(g[0].z) + fbits(g[1].z)) + (fbits(g[2].z) + fbits(g[3].z)));
    const u32 cbp = pk((fbits(g[4].x) + fbits(g[5].x)) + (fbits(g[6].x) + fbits(g[7].x)), (fbits(g[4].z) + fbits(g[5].z)) + (fbits(g[6].z) + fbits(g[7].z)));
    lds_st<u32>(L_V + t4 * 4, pk(1.f + lo16(cbp), 1.f + hi16(cbp)));
    lds_st<u32>(L_V + 1024 + t4 * 4, sbp);
    float m0 = fbits(g[8].x), m1 = fbits(g[8].z);
    m0 += dppf<0xB1>(m0); m1 += dppf<0xB1>(m1);
    m0 += dppf<0x4E>(m0); m1 += dppf<0x4E>(m1);
    if (t4 < 128 && (t4 & 3) == 0) lds_st<f2>(L_GB + (t4 >> 2) * 8, f2{rbf(m0), rbf(m1)});
    STAMP(9);
    BAR();
    BAR();
    conv_epilogue(1);
  }
  STAMP(10);
  if (w < 7) { READ_W(5, 6); }
  BAR();
  if (w < 4) {
#pragma unroll
    for (int u = 0; u < 5; u++) {
      stage_w(rwg, rwu, svo0, 11 + u, u, w, gj);
      if (w < 3) stage_w(rwg, rwu, svo1, 11 + u, u, w + 4, gj);
    }
  }
  {
    const u32 sv = lds_ld<u16>(L_V + tid * 2), e = tid & 7, sh = sv << ((e & 1) * 16);
    lds_st<v4u>(L_DIAG + (tid >> 4) * 336 + (tid & 15) * 16, v4u{(e >> 1) == 0 ? sh : 0u, (e >> 1) == 1 ? sh : 0u, (e >> 1) == 2 ? sh : 0u, (e >> 1) == 3 ? sh : 0u});
    if (tid < 128) lds_st<v4u>(L_DIAG + (tid >> 2) * 336 + 256 + (tid & 3) * 16, lds_ld<v4u>(L_V + ((tid >> 1) & 1) * 1024 + (tid >> 2) * 32 + (tid & 1) * 16));
    if (tid < 32) lds_st<v4u>(L_DIAG + tid * 336 + 320, v4u{0u, 0u, 0u, 0u});
  }
  auto wait_x1 = [&](int half) {
    for (u32 spin = 0;; spin++) {
      v2u f0, f1, f2_, f3;
      const u32 so = WS_F2 + half * 65536 + xcd * 8192;
      LD64(f0, rws, l * 32, so, "sc1");
      LD64(f1, rws, l * 32, so + 2048, "sc1");
      LD64(f2_, rws, l * 32, so + 4096, "sc1");
      LD64(f3, rws, l * 32, so + 6144, "sc1");
      asm volatile("s_waitcnt vmcnt(0)" : "+v"(f0), "+v"(f1), "+v"(f2_), "+v"(f3)::"memory");
      const bool ok = f0.x == tagA && f0.y == tagB && f1.x == tagA && f1.y == tagB && f2_.x == tagA && f2_.y == tagB && f3.x == tagA && f3.y == tagB;
      if (__all(ok) || spin > kSpin) break;
      __builtin_amdgcn_s_sleep(4);
    }
  };
  if (w >= 4) { VM0(); publish_x1(1); }
  BAR();
  STAMP(11);
  f2 stv = {0.f, 0.f};
  const u32 dga = L_DIAG + ((l & 31) < 16 ? (((l >> 3) & 1) == (l >> 5) ? (l & 15) * 16 : 320) : ((l & 31) < 18 ? 256 + (l & 1) * 32 + (l >> 5) * 16 : 320));
#define PREP_W(s0, s1)                                                                       \
  _Pragma("unroll") for (int s = (s0); s < (s1); s++) {                                      \
    const f16v d = mfma32(lds_ld<v4u>(dga + s * 336), W[s], f16v{});                         \
    W[s] = v4u{pk(d[0], d[1]), pk(d[2], d[3]), pk(d[4], d[5]), pk(d[6], d[7])};              \
    stv += f2{d[8], d[9]};                                                                   \
  }
  const u32 tvo = (l >> 3) * 128 + (((l & 7) ^ (((l >> 4) & 1) << 2)) << 4);
  if (w < 7) { PREP_W(0, 22); VM0(); }
  else wait_x1(0);
  STAMP(12);
  BAR();
#pragma unroll
  for (int k = 0; k < 8; k++) DMA(rws, tvo, WS_X1T + gi * 65536 + (w * 8 + k) * 1024, L_TA + (w * 8 + k) * 1024, "sc1");
  if (w < 7) {
    READ_W(11, 5);
    PREP_W(22, 32);
    if (l < 32) {
      lds_st<float>(L_ST + w * 256 + l * 4, stv.x);
      lds_st<float>(L_ST + w * 256 + 128 + l * 4, (l & 8) ? stv.y : stv.y * -1.44269504088896341f);
    }
  }
  STAMP(13);
  VM0();
  BAR();
  STAMP(14);

  const u32 tra = ((l >> 5) * 4 + ((l >> 2) & 3)) * 128 + ((((l >> 4) & 1) * 32 + (l & 3) * 8) ^ (((l >> 3) & 1) << 6));
  const u32 rra = ((gj & 7) * 64 + (w >> 1) * 16 + (l >> 4) * 4 + ((l >> 2) & 3)) * 128 + (((gj >> 3) * 64 + (w & 1) * 32 + (l & 3) * 8) ^ (((l >> 3) & 1) << 6));
  const rsrc_t rwd = mk(p.wd, kF * kD * 2);
  const u32 dvo = (l >> 3) * 1024 + (((l & 7) ^ (((l >> 4) & 1) << 2)) << 4);
  const u32 d1base = w < 6 ? L_X + w * 10240 : 138752 + (w - 6) * 10240;
  f16v acc[2];
  auto gemm1 = [&](u32 tb, int s0, int s1) {
#pragma unroll
    for (int s = 0; s < 32; s++)
      if (s >= s0 && s < s1) {
        acc[0] = mfma32(W[s], tr_frag(tb + tra + s * 2048, tb + tra + s * 2048 + 1024), acc[0]);
        acc[1] = mfma32(W[s], tr_frag(tb + (tra ^ 64) + s * 2048, tb + (tra ^ 64) + s * 2048 + 1024), acc[1]);
      }
  };
  auto ln2_stats = [&](u32 tb, int half) {
#pragma unroll
    for (int nt = 0; nt < 2; nt++) {
      float s = 0.f, q = 0.f, s2 = 0.f, q2 = 0.f;
      const u32 a = tb + (tra ^ (nt * 64));
#pragma unroll 8
      for (int k = 0; k < 32; k++) {
        const v2u x = lds_tr(a + k * 2048), y = lds_tr(a + k * 2048 + 1024);
        s = dot2(x.x, 0x3f803f80u, s); q = dot2(x.x, x.x, q); s2 = dot2(x.y, 0x3f803f80u, s2); q2 = dot2(x.y, x.y, q2);
        s = dot2(y.x, 0x3f803f80u, s); q = dot2(y.x, y.x, q); s2 = dot2(y.y, 0x3f803f80u, s2); q2 = dot2(y.y, y.y, q2);
      }
      auto sw = __builtin_amdgcn_permlane32_swap(ubits(s + s2), ubits(q + q2), false, false);
      const float t = fbits(sw[0]) + fbits(sw[1]);
      auto s3 = __builtin_amdgcn_permlane32_swap(ubits(t), ubits(t), false, false);
      const float mean = fbits(s3[0]) * (1.f / kD);
      const float var = rbf(fmaxf(fbits(s3[1]) * (1.f / kD) - mean * mean, 0.f));
      const float den = rbf(__builtin_amdgcn_sqrtf(rbf(var + 1e-5f)));
      if (l < 32) lds_st<f2>(L_STAT + half * 512 + (nt * 32 + l) * 8, f2{rbf(mean), __builtin_amdgcn_rcpf(den)});
    }
  };
  auto glu_epilogue = [&](int half) {
    f4 S[4], T[4];
#pragma unroll
    for (int k = 0; k < 4; k++) {
      S[k] = lds_ld<f4>(L_ST + w * 256 + k * 32 + (l >> 5) * 16);
      T[k] = lds_ld<f4>(L_ST + w * 256 + 128 + k * 32 + (l >> 5) * 16);
    }
#pragma unroll
    for (int nt = 0; nt < 2; nt++) {
      const f2 st = lds_ld<f2>(L_STAT + half * 512 + (nt * 32 + (l & 31)) * 8);
      const float rg = st.y * -1.44269504088896341f;
      float o[8];
#pragma unroll
      for (int k = 0; k < 2; k++)
#pragma unroll
        for (int e = 0; e < 4; e++) {
          const float z = (acc[nt][8 * k + e] - st.x * S[2 * k][e]) * rg + T[2 * k][e];
          const float u = (acc[nt][8 * k + 4 + e] - st.x * S[2 * k + 1][e]) * st.y + T[2 * k + 1][e];
          o[4 * k + e] = u * __builtin_amdgcn_rcpf(1.f + __builtin_amdgcn_exp2f(z));
        }
      st128_dev(v4u{pk(o[0], o[1]), pk(o[2], o[3]), pk(o[4], o[5]), pk(o[6], o[7])}, rws, l * 16,
                WS_HH + ((half * 32 + 2 * gi + nt) * 112 + 7 * gj + w) * 1024);
    }
  };
  auto publish_hh = [&](int half) {
    u32 old = 0;
    if (l == 0) old = __hip_atomic_fetch_add((LAS u32*)(size_t)(M_CNT + 8 + half * 4), 1u, __ATOMIC_RELAXED, __HIP_MEMORY_SCOPE_WORKGROUP);
    if (__builtin_amdgcn_readfirstlane(old) == 6 && l == 0) st64_dev(v2u{tagA, tagB}, rws, 0, WS_F3 + ((half * 16 + gi) * 16 + gj) * 128);
  };
  auto stage_d = [&](int s0, int n, u32 base) {
#pragma unroll
    for (int k = 0; k < 2 * n; k++) DMA(rwd, dvo, (w * 224 + s0 * 16 + k * 8) * 1024 + (gj & 7) * 128, base + k * 1024, "");
  };
  const v2u resa = lds_tr(L_TA + rra);
  if (w < 7) {
    acc[0] = acc[1] = f16v{};
    gemm1(L_TA, 0, 32);
  } else {
    wait_x1(1);
#pragma unroll 8
    for (int k = 0; k < 64; k++) DMA(rws, tvo, WS_X1T + (16 + gi) * 65536 + k * 1024, L_TB + k * 1024, "sc1");
    ln2_stats(L_TA, 0);
    VM0();
  }
  STAMP(15);
  BAR();
  const v2u resb = lds_tr(L_TB + rra);
  if (w < 7) {
    glu_epilogue(0);
    acc[0] = acc[1] = f16v{};
    gemm1(L_TB, 0, 8);
    VM0();
    publish_hh(0);
    stage_d(0, 5, d1base);
    gemm1(L_TB, 8, 32);
  } else {
    stage_d(0, 5, d1base);
    ln2_stats(L_TB, 1);
  }
  STAMP(16);
  BAR();
  if (w < 7) glu_epilogue(1);
  VM0();
  if (w < 7) publish_hh(1);
  STAMP(17);

  v4u Wd[14][2];
#pragma unroll
  for (int s = 0; s < 5; s++)
#pragma unroll
    for (int mt = 0; mt < 2; mt++) Wd[s][mt] = tr_frag(d1base + (tra ^ (mt * 64)) + s * 2048, d1base + (tra ^ (mt * 64)) + s * 2048 + 1024);
  BAR();
  const u32 d2base = L_X + w * 18432;
  stage_d(5, 9, d2base);
  v4u hf[14];
  auto load_hh = [&](int half) {
    for (u32 spin = 0;; spin++) {
      v2u f;
      LD64(f, rws, (l & 1) * 128, WS_F3 + ((half * 16 + gi) * 16 + 2 * w) * 128, "sc1");
      asm volatile("s_waitcnt vmcnt(0)" : "+v"(f)::"memory");
      if (__all(f.x == tagA && f.y == tagB) || spin > kSpin) break;
      __builtin_amdgcn_s_sleep(4);
    }
#pragma unroll
    for (int s = 0; s < 14; s++)
      hf[s] = __builtin_amdgcn_raw_buffer_load_b128(rws, l * 16 + (s & 3) * 1024, WS_HH + ((half * 32 + 2 * gi + (gj >> 3)) * 112 + 14 * w + (s & ~3)) * 1024, 16);
  };
  auto gemm2 = [&](u32 red) {
    acc[0] = acc[1] = f16v{};
#pragma unroll
    for (int s = 0; s < 14; s++) {
      acc[0] = mfma32(Wd[s][0], hf[s], acc[0]);
      acc[1] = mfma32(Wd[s][1], hf[s], acc[1]);
    }
#pragma unroll
    for (int mt = 0; mt < 2; mt++)
#pragma unroll
      for (int g = 0; g < 4; g++)
        lds_st<f4>(red + w * 8192 + (mt * 4 + g) * 1024 + l * 16, f4{acc[mt][4 * g], acc[mt][4 * g + 1], acc[mt][4 * g + 2], acc[mt][4 * g + 3]});
  };
  const rsrc_t rout = mk(p.out, kL * kD * 2);
  auto out_epilogue = [&](int half, u32 red, v2u res) {
    const u32 a = red + ((w >> 1) * 4 + (l >> 4)) * 512 + ((w & 1) * 16 + (l & 15)) * 16;
    f4 s = lds_ld<f4>(a);
#pragma unroll
    for (int k = 1; k < 8; k++) s += lds_ld<f4>(a + k * 8192);
    const f4 gb = lds_ld<f4>(L_GB + ((w >> 1) * 4 + (l >> 4)) * 16);
    const u32 r01 = pk(s.x, s.y), r23 = pk(s.z, s.w);
    const u32 z01 = pk(gb.x * lo16(r01), gb.y * hi16(r01)), z23 = pk(gb.z * lo16(r23), gb.w * hi16(r23));
    const v2u o = {pk(lo16(res.x) + lo16(z01), hi16(res.x) + hi16(z01)), pk(lo16(res.y) + lo16(z23), hi16(res.y) + hi16(z23))};
    __builtin_amdgcn_raw_buffer_store_b64(o, rout, ((w & 1) * 16 + (l & 15)) * 1024 + ((w >> 1) * 4 + (l >> 4)) * 8,
                                          (half * 1024 + gi * 64 + (gj >> 3) * 32) * 1024 + (gj & 7) * 128, 0);
  };
  load_hh(0);
  STAMP(18);
#pragma unroll
  for (int s = 5; s < 14; s++)
#pragma unroll
    for (int mt = 0; mt < 2; mt++) Wd[s][mt] = tr_frag(d2base + (tra ^ (mt * 64)) + (s - 5) * 2048, d2base + (tra ^ (mt * 64)) + (s - 5) * 2048 + 1024);
  BAR();
  gemm2(L_X);
  STAMP(19);
  load_hh(1);
  STAMP(20);
  BAR();
  out_epilogue(0, L_X, resa);
  gemm2(L_STG);
  STAMP(21);
  BAR();
  out_epilogue(1, L_STG, resb);
  if (w == 0) { u64 t0_, t1_;
      asm volatile("s_memrealtime %0\n\ts_waitcnt lgkmcnt(0)" : "=s"(t0_)::"memory");
      do { asm volatile("s_memrealtime %0\n\ts_waitcnt lgkmcnt(0)" : "=s"(t1_)::"memory"); } while (t1_ - t0_ < AMPK * (e1 - e0)); }
}

}

extern "C" void kernel_launch(void* const* d_in, const int* in_sizes, int n_in, void* d_out, int out_size, void* d_ws,
                              size_t ws_size, hipStream_t stream) {
  using namespace s4glu;
  (void)in_sizes; (void)n_in; (void)out_size; (void)ws_size;
  Params p;
  p.x = d_in[0]; p.are = d_in[1]; p.aim = d_in[2]; p.bre = d_in[3]; p.bim = d_in[4]; p.cre = d_in[5]; p.cim = d_in[6];
  p.logdt = d_in[7]; p.dskip = d_in[8]; p.wg = d_in[10]; p.wu = d_in[11]; p.wd = d_in[12]; p.c = d_in[13]; p.wmod = d_in[14];
  p.out = d_out; p.ws = d_ws;
  (void)hipFuncSetAttribute((const void*)fused_kernel, hipFuncAttributeMaxDynamicSharedMemorySize, L_END);
  fused_kernel<<<256, 512, L_END, stream>>>(p);
}
```

```cpp
#include <hip/hip_runtime.h>
#include <stdint.h>

#define DBG 0

namespace s4glu {

typedef unsigned short u16;
typedef unsigned u32;
typedef unsigned long long u64;
typedef u32 v4u __attribute__((ext_vector_type(4)));
typedef u32 v2u __attribute__((ext_vector_type(2)));
typedef float f2 __attribute__((ext_vector_type(2)));
typedef float f4 __attribute__((ext_vector_type(4)));
typedef float f16v __attribute__((ext_vector_type(16)));
typedef __bf16 bf2 __attribute__((ext_vector_type(2)));
typedef __bf16 bf8 __attribute__((ext_vector_type(8)));
typedef _Float16 h2 __attribute__((ext_vector_type(2)));
typedef _Float16 h8 __attribute__((ext_vector_type(8)));
typedef short s4v __attribute__((ext_vector_type(4)));
typedef __amdgpu_buffer_rsrc_t rsrc_t;
#define LAS __attribute__((address_space(3)))
#define CAS __attribute__((address_space(4)))
#define DEV __device__ __forceinline__

constexpr int kL = 2048, kD = 512, kN = 128, kF = 1792;
constexpr float kLog2e = 1.44269504088896341f;

constexpr u32 WS_NONCE = 0;
constexpr u32 WS_STG = 4096;
constexpr u32 WS_MODG = WS_STG + 16384;
constexpr u32 WS_F2 = WS_MODG + 98304;
constexpr u32 WS_F3 = WS_F2 + 131072;
constexpr u32 WS_S2 = WS_F3 + 32 * 28 * 128;
constexpr u32 WS_MODF = WS_S2 + 16384;
constexpr u32 WS_XG = 0x80000;
constexpr u32 WS_X1T = WS_XG + 4194304;
constexpr u32 WS_X1S = WS_X1T + 2097152;
constexpr u32 WS_HH = WS_X1S + 2097152;
constexpr u32 WS_END = WS_HH + 7340032;
constexpr u32 WS_DBG = WS_END;
static_assert(WS_MODF + 6144 <= WS_XG, "ws map");

constexpr u32 L_MODP = 0;
constexpr u32 L_V = 0;
constexpr u32 L_STATP = 3072;
constexpr u32 L_RES = L_STATP;
constexpr u32 L_G = L_STATP;
constexpr u32 L_GB = 7168;
constexpr u32 L_MISC = 7424;
constexpr u32 L_ST = 163840 - 256 - 2048;
constexpr u32 L_STAMP = 7680;
constexpr u32 L_X = 8448;
constexpr u32 L_FACT = L_X;
constexpr u32 L_KC = L_X;
constexpr u32 L_HR = L_KC + 33280;
constexpr u32 L_XB = L_HR + 9216;
constexpr u32 L_P = L_XB + 8192;
constexpr u32 L_T0 = L_X;
constexpr u32 L_STG = L_X + 67072;
constexpr u32 L_T1 = L_STG;
constexpr u32 L_ROW = L_STG + 65536;
constexpr u32 L_D0 = L_STG + 65536;
constexpr u32 L_END = 163840;
static_assert(L_STG + 5 * 16384 <= L_END && L_D0 + 16384 <= L_END && L_X + 8 * 16384 <= L_END, "lds map");
constexpr u32 M_CNT = L_MISC;
constexpr u32 M_INV = L_MISC + 16;
constexpr u32 M_XA = L_MISC + 32;
constexpr u32 L_DUMP = L_END - 256;

constexpr u32 kSpin = 40000;
constexpr u32 kOOB = 0x80000000u;

struct Params {
  const void *x, *are, *aim, *bre, *bim, *cre, *cim, *logdt, *dskip, *wg, *wu, *wd, *c, *wmod;
  void *out, *ws;
};

DEV rsrc_t mk(const void* p, u32 bytes) { return __builtin_amdgcn_make_buffer_rsrc((void*)p, 0, bytes, 0x00020000); }
DEV float lo16(u32 v) { return __builtin_bit_cast(float, v << 16); }
DEV float hi16(u32 v) { return __builtin_bit_cast(float, v & 0xffff0000u); }
DEV f2 up2(u32 v) { return f2{lo16(v), hi16(v)}; }
DEV u32 pk2(f2 t) { return __builtin_bit_cast(u32, __builtin_convertvector(t, bf2)); }
DEV u32 pk(float a, float b) { return pk2(f2{a, b}); }
DEV u32 pkh(float a, float b) { f2 t = {a, b}; return __builtin_bit_cast(u32, __builtin_convertvector(t, h2)); }
DEV float rbf(float a) { return lo16(pk(a, a)); }
DEV float fbits(u32 v) { return __builtin_bit_cast(float, v); }
DEV u32 ubits(float v) { return __builtin_bit_cast(u32, v); }
DEV f2 fma2(f2 a, f2 b, f2 c) { return __builtin_elementwise_fma(a, b, c); }
DEV float dot2(u32 a, u32 b, float c) {
  return __builtin_amdgcn_fdot2_f32_bf16(__builtin_bit_cast(bf2, a), __builtin_bit_cast(bf2, b), c, false);
}
template <int CTRL, int RM = 0xf> DEV float dppf(float v) {
  return fbits(__builtin_amdgcn_update_dpp(0u, ubits(v), CTRL, RM, 0xf, true));
}
template <int CTRL, int RM = 0xf> DEV u32 dppu(u32 v) { return __builtin_amdgcn_update_dpp(0u, v, CTRL, RM, 0xf, true); }
DEV u32 umax(u32 a, u32 b) { return a > b ? a : b; }
DEV u32 wave_umax(u32 v) {
  v = umax(v, dppu<0xB1>(v));
  v = umax(v, dppu<0x4E>(v));
  v = umax(v, dppu<0x141>(v));
  v = umax(v, dppu<0x140>(v));
  v = umax(v, dppu<0x142, 0xa>(v));
  v = umax(v, dppu<0x143, 0xc>(v));
  return __builtin_amdgcn_readlane(v, 63);
}

template <class T> DEV T lds_ld(u32 off) { return *(LAS T*)(size_t)(off); }
template <class T> DEV T lds_ldv(u32 off) { return *(volatile LAS T*)(size_t)(off); }
template <class T> DEV void lds_stv(u32 off, T v) { asm volatile("" ::: "memory"); *(volatile LAS T*)(size_t)(off) = v; }
template <class T> DEV void lds_st(u32 off, T v) { *(LAS T*)(size_t)(off) = v; }
DEV void lds_add(u32 off, u32 v) {
  asm volatile("s_mov_b64 exec, 1\n\tds_add_u32 %0, %1\n\ts_mov_b64 exec, -1" ::"v"(off), "v"(v) : "memory");
}
DEV u32 lds_ticket(u32 off) {
  u32 v, r;
  asm volatile("s_mov_b64 exec, 1\n\tds_add_rtn_u32 %0, %2, %3\n\ts_mov_b64 exec, -1\n\ts_waitcnt lgkmcnt(0)\n\tv_readfirstlane_b32 %1, %0"
               : "=&v"(v), "=s"(r) : "v"(off), "v"(1u) : "memory");
  return r;
}
DEV void lds_wait(u32 off, u32 n) {
#pragma unroll 1
  for (u32 spin = 0; __builtin_amdgcn_readfirstlane(*(volatile LAS u32*)(size_t)off) < n && spin < kSpin * 8; spin++) {}
  asm volatile("" ::: "memory");
}
DEV void lds_wait4(u32 l4, u32 off, u32 t) {
  if (__builtin_amdgcn_ballot_w64(*(volatile LAS u32*)(size_t)(l4 + off) != t)) {
    u32 spin = 0;
#pragma unroll 1
    do { __builtin_amdgcn_s_sleep(1); } while (__builtin_amdgcn_ballot_w64(*(volatile LAS u32*)(size_t)(l4 + off) != t) && ++spin < kSpin * 8);
  }
  asm volatile("" ::: "memory");
}
DEV void lds_wait4x2(u32 l4, u32 o0, u32 o1, u32 t) {
  auto bad = [&]() { const u32 a = *(volatile LAS u32*)(size_t)(l4 + o0), c = *(volatile LAS u32*)(size_t)(l4 + o1); return ((a ^ t) | (c ^ t)) != 0u; };
  if (__builtin_amdgcn_ballot_w64(bad())) {
    u32 spin = 0;
#pragma unroll 1
    do { __builtin_amdgcn_s_sleep(1); } while (__builtin_amdgcn_ballot_w64(bad()) && ++spin < kSpin * 8);
  }
  asm volatile("" ::: "memory");
}
DEV v2u lds_tr(u32 off) {
  return __builtin_bit_cast(v2u, __builtin_amdgcn_ds_read_tr16_b64_v4i16((LAS s4v*)(size_t)(off)));
}
DEV v4u tr_frag(u32 a0, u32 a1) { v2u x = lds_tr(a0), y = lds_tr(a1); return v4u{x.x, x.y, y.x, y.y}; }
DEV v4u tr_frag(u32 a0, u32 a1, int off) { return tr_frag(a0 + off, a1 + off + 1024); }
DEV f16v mfma32(v4u a, v4u b, f16v c) {
  return __builtin_amdgcn_mfma_f32_32x32x16_bf16(__builtin_bit_cast(bf8, a), __builtin_bit_cast(bf8, b), c, 0, 0, 0);
}
DEV f4 mfma16(v4u a, v4u b, f4 c) {
  return __builtin_amdgcn_mfma_f32_16x16x32_bf16(__builtin_bit_cast(bf8, a), __builtin_bit_cast(bf8, b), c, 0, 0, 0);
}
DEV f4 mfma16h(v4u a, v4u b, f4 c) {
  return __builtin_amdgcn_mfma_f32_16x16x32_f16(__builtin_bit_cast(h8, a), __builtin_bit_cast(h8, b), c, 0, 0, 0);
}

#define SB() __builtin_amdgcn_sched_barrier(0)
#define BAR() asm volatile("s_waitcnt lgkmcnt(0)\n\ts_barrier" ::: "memory")
#define VMW(n) asm volatile("s_waitcnt vmcnt(" #n ")" ::: "memory")
#define PIN(x) asm volatile("" : "+v"(x))
#define PINS(x) do { x = __builtin_amdgcn_readfirstlane(x); asm volatile("" : "+s"(x)); } while (0)
#define LD128(dst, rs, vo, so, mods) \
  asm volatile("buffer_load_dwordx4 %0, %1, %2, %3 offen " mods : "=v"(dst) : "v"(vo), "s"(rs), "s"(so) : "memory")
#define LD64(dst, rs, vo, so, mods) \
  asm volatile("buffer_load_dwordx2 %0, %1, %2, %3 offen " mods : "=v"(dst) : "v"(vo), "s"(rs), "s"(so) : "memory")
#define LD16(dst, rs, vo, so, mods) \
  asm volatile("buffer_load_ushort %0, %1, %2, %3 offen " mods : "=v"(dst) : "v"(vo), "s"(rs), "s"(so) : "memory")
#define DMA(rs, vo, so, m, mods)                                                                          \
  asm volatile("s_mov_b32 m0, %3\n\ts_nop 0\n\tbuffer_load_dwordx4 %0, %1, %2 offen " mods " lds" ::"v"(vo), \
               "s"(rs), "s"(so), "s"(m)                                                                   \
               : "memory")
#define DMA4(rs, vo, so, m, mods)                                                                  \
  asm volatile("s_mov_b32 m0, %3\n\ts_nop 0\n\tbuffer_load_dwordx4 %0, %1, %2 offen " mods         \
               " lds\n\tbuffer_load_dwordx4 %0, %1, %2 offen offset:1024 " mods                    \
               " lds\n\tbuffer_load_dwordx4 %0, %1, %2 offen offset:2048 " mods                    \
               " lds\n\tbuffer_load_dwordx4 %0, %1, %2 offen offset:3072 " mods " lds" ::"v"(vo),  \
               "s"(rs), "s"(so), "s"(m)                                                            \
               : "memory")
#define TOUCH(rs, vo, so) \
  asm volatile("s_mov_b32 m0, %3\n\ts_nop 0\n\tbuffer_load_dword %0, %1, %2 offen lds" ::"v"(vo), "s"(rs), "s"(so), "s"(L_DUMP) : "memory")
DEV void st128_dev(v4u v, rsrc_t rs, u32 vo, u32 so) {
  asm volatile("buffer_store_dwordx4 %0, %1, %2, %3 offen sc1\n\ts_nop 1" ::"v"(v), "v"(vo), "s"(rs), "s"(so) : "memory");
}
DEV void st64_dev(v2u v, rsrc_t rs, u32 vo, u32 so) {
  asm volatile("buffer_store_dwordx2 %0, %1, %2, %3 offen sc1" ::"v"(v), "v"(vo), "s"(rs), "s"(so) : "memory");
}
DEV u32 xo(u32 a, u32 b, u32 c) { return __builtin_amdgcn_bitop3_b32(a, b, c, 0xBE); }
DEV u32 tag_bad(v4u g, u32 ta, u32 tb, u32 c = 0u) { return xo(g.y, ta, xo(g.w, tb, c)); }
DEV u32 tag_bad(v2u g, u32 ta, u32 tb, u32 c = 0u) { return xo(g.x, ta, xo(g.y, tb, c)); }
DEV bool any_set(u32 v) { return __builtin_expect(__builtin_amdgcn_ballot_w64(v != 0u) != 0ull, 0); }
#define RETRY(cond, body)                                  \
  if (cond) {                                              \
    u32 spin = 0;                                          \
    _Pragma("unroll 1") do { body } while ((cond) && ++spin < kSpin); \
  }

#define ZOOM 0
#define EXP 0
#define GDELAY 0
#define RSTEP 18
#define PSLEEP 6
#define Q0POS 2
#define SPREAD 1
#define HPRIO 0
#define GSYNC 80
#define ROT 1
#define CODE_LINES 256
#define WTOUCH 1
#define CYC 0
#define Z(z, i) do { if (ZOOM == (z)) STAMP_(i); } while (0)
#define ZC(i) do { if (ZOOM == 9) { u64 a_, b_; asm volatile("s_memtime %0\n\ts_memrealtime %1\n\ts_waitcnt lgkmcnt(0)" : "=s"(a_), "=s"(b_)::"memory"); \
  if (l == 0) { lds_st<u32>(L_STAMP + (w * 24 + 2 * (i) + 1) * 4, (u32)b_); lds_st<u32>(L_STAMP + (w * 24 + 2 * (i) + 2) * 4, (u32)a_); } } } while (0)
#define ZS(i) do { if (ZOOM == 5 && c == 1) asm volatile("s_memtime %0\n\ts_waitcnt lgkmcnt(0)" : "=s"(zs[i])::"memory"); } while (0)
#define STAMP(i) do { if (ZOOM == 0 || (i) == 0 || (i) == 22) STAMP_(i); } while (0)
#define STAMP_(i)

DEV f2 cmul(f2 a, f2 b) { return fma2(a.yy, f2{-b.y, b.x}, a.xx * b); }
DEV f2 cexpv(float al, float be, float v) {
  const float e = __builtin_amdgcn_exp2f(al * v);
  const float r = __builtin_amdgcn_fractf(be * v);
  return f2{__builtin_amdgcn_cosf(r), __builtin_amdgcn_sinf(r)} * e;
}
DEV float exp_acc(float x) {
  const float t = x * kLog2e;
  const float r = __builtin_fmaf(x, 1.92596299112661746e-8f, __builtin_fmaf(x, kLog2e, -t));
  const float e = __builtin_amdgcn_exp2f(t);
  return __builtin_fmaf(e * r, 0.693147180559945309f, e);
}

__global__ void __launch_bounds__(512, 2) fused_kernel(Params p) {
  asm volatile("" ::"s"(p.x), "s"(p.ws));
  const int tid = threadIdx.x, l = tid & 63;
  const int w = __builtin_amdgcn_readfirstlane(tid >> 6);
  const int b = blockIdx.x, xcd = b & 7, r = b >> 3;
  const rsrc_t rws = mk(p.ws, WS_END);
  STAMP(0); ZC(0);

  const u32 l16 = l * 16;
  const int t4 = tid & 255, w4 = w & 3;
  const int cp = xcd * 32 + r;
  const int g4 = xcd & 3, jd = r & 7;
  const int gI = (xcd >> 2) * 4 + (r >> 3), gJ = g4 * 7 + jd;
  const bool active = jd < 7, ismod = (r & 3) != 0;
  v4u row[2], wm[4];
  v2u cv;
  u64 nonce;
  u32 pAr, pAi, pBr, pBi, pCr, pCi;
  const int mi = (r >> 2) * 3 + (r & 3) - 1, mkq = mi & 3, mgrp = mi < 8 ? xcd * 2 + (mi >> 2) : 14 + xcd * 4 + (mi >> 2);
  if (w < 4) {
    const rsrc_t rx = mk(p.x, kL * kD * 2);
    LD128(row[0], rx, l16, (b * 8 + 2 * w) * 1024, "nt");
    LD128(row[1], rx, l16, (b * 8 + 2 * w) * 1024, "offset:1024 nt");
    if (ismod) {
      const rsrc_t rm = mk(p.wmod, kD * 6 * kD * 2);
      LD64(cv, mk(p.c, kD * 2), (t4 >> 3) * 8, mkq * 256, "");
      const u32 vm = (t4 >> 3) * 24576 + (t4 & 7) * 16, sm = mkq * 128 * 6144 + mgrp * 128;
#pragma unroll
      for (int k = 0; k < 4; k++) LD128(wm[k], rm, vm, sm + k * 6144, "nt");
    }
  } else {
    const int ch = w4 >> 1, mode = (w4 & 1) * 64 + l;
    {
      const u32 vo = mode * 2, so = (2 * cp + ch) * 256;
      LD16(pAr, mk(p.are, kD * kN * 2), vo, so, "nt");
      LD16(pAi, mk(p.aim, kD * kN * 2), vo, so, "nt");
      LD16(pBr, mk(p.bre, kD * kN * 2), vo, so, "nt");
      LD16(pBi, mk(p.bim, kD * kN * 2), vo, so, "nt");
      LD16(pCr, mk(p.cre, kD * kN * 2), vo, so, "nt");
      LD16(pCi, mk(p.cim, kD * kN * 2), vo, so, "nt");
    }
    if (CODE_LINES && w == 7) {
      u64 pc;
      asm volatile("s_getpc_b64 %0" : "=s"(pc));
      TOUCH(mk((const void*)pc, CODE_LINES * 128), (l < CODE_LINES / 32 ? (l * 32 + r) * 128 : kOOB), 0u);
    }
  }
  asm volatile("s_load_dwordx2 %0, %1, 0x0" : "=s"(nonce) : "s"(p.ws) : "memory");
  const int kr = ROT ? r >> 3 : 0;
  const int wt = ROT ? (w & 4) | ((w + jd) & 3) : w;
  const int wa = (wt + 2 * kr) & 7;
  const int wk = ROT ? (w + jd + 2 * (r >> 3)) & 7 : w;
  const u32 l4 = (l & 3) * 4;
  u32 v0 = l == 0 ? 0u : kOOB;
  const rsrc_t rwg = mk(p.wg, kD * kF * 2), rwu = mk(p.wu, kD * kF * 2);
  const u32 svg = (l >> 3) * 3584 + (((l & 7) ^ ((l >> 3) & 2)) << 4), svu = svg ^ 64;
  auto stage_w = [&](int q) {
    u32 so;
#define STG_G(dm, ds) "buffer_load_dwordx4 %1, %3, %0 offen lds\n\ts_add_u32 m0, m0, " dm "\n\ts_add_u32 %0, %0, " ds "\n\t"
#define STG_U(dm, ds) "buffer_load_dwordx4 %2, %4, %0 offen lds\n\ts_add_u32 m0, m0, " dm "\n\ts_add_u32 %0, %0, " ds "\n\t"
#define STG_UNIT STG_G("0x400", "0x7000") STG_G("0x2000", "0") STG_U("0xfffffc00", "0xffff9000") STG_U("0x2000", "0x38000")
    asm volatile("s_mov_b32 m0, %5\n\ts_mov_b32 %0, %6\n\t" STG_UNIT STG_UNIT
                 : "=&s"(so)
                 : "v"(svg), "v"(svu), "s"(rwg), "s"(rwu), "s"(L_STG + (q & 1) * 32768 + w4 * 2048), "s"((((q + kr) & 3) * 128 + w4 * 16) * 3584 + gJ * 128)
                 : "memory", "scc");
  };
  u32 tagA, tagB;
  auto take_tags = [&]() {
    asm volatile("s_waitcnt lgkmcnt(0)" : "+s"(nonce)::"memory");
    nonce += 0x9E3779B97F4A7C15ull;
    tagA = (u32)nonce; tagB = (u32)(nonce >> 32);
  };
  v4u g9;
  v2u gf[3];
  const u32 vg9 = ((l & 4) * 128 + 512 + cp) * 64 + (l & 3) * 16;
#define GATHER2()                                                     \
  LD64(gf[0], rws, t4 * 8, WS_MODF, "sc1");                           \
  LD64(gf[1], rws, t4 * 8, WS_MODF + 2048, "sc1");                    \
  LD64(gf[2], rws, (l & 31) * 8, WS_MODF + 4096 + jd * 256, "sc1")
#define GATHER2_PIN() asm volatile("" : "+v"(gf[0]), "+v"(gf[1]), "+v"(gf[2])::"memory")

  v4u W[32];
  const int mt = w & 3, nh = w >> 2;
  u32 wtr = L_STG + ((l >> 1) & 1) * 8192 + ((l >> 5) * 8 + ((l >> 2) & 3)) * 128 +
            (((2 * mt + ((l >> 4) & 1)) ^ ((l >> 2) & 2) ^ ((l << 1) & 4)) << 4) + (l & 1) * 8;
#define READ_W(q)                                                                                \
  _Pragma("unroll") for (int k = 0; k < 8; k++)                                                  \
      W[8 * (q) + k] = tr_frag(wtr + (((q) & 1) * 2 + (k >> 2)) * 16384 + (k & 3) * 2048, wtr + (((q) & 1) * 2 + (k >> 2)) * 16384 + (k & 3) * 2048 + 512)
  v2u fx;
  u32 sF2 = WS_F2 + xcd * 8192 + wa * 1024;
#define POLL_X1(half) LD64(fx, rws, vfl, sF2 + (half) * 65536, "sc1")
#define RETRY_X1(half) PIN(fx); RETRY(any_set(tag_bad(fx, tagA, tagB)), __builtin_amdgcn_s_sleep(PSLEEP); POLL_X1(half); VMW(0); PIN(fx);)
  auto flag_x1 = [&](int half) { st64_dev(v2u{tagA, tagB}, rws, l < 8 ? l * 8192 : kOOB, WS_F2 + half * 65536 + cp * 32); };
  const int tb0 = 2 * gI;
  u32 aI = gI, a4 = g4, aD = jd, aJ = gJ, aK = wk, aA = wa, aR = ROT ? jd + 2 * (r >> 3) : 0;
#define sRes (WS_X1T + (2 * aI + (a4 >> 1)) * 65536 + (aD * 64 + w4 * 16) * 128 + (a4 & 1) * 64)
#define sF3 (WS_F3 + (2 * aI + (a4 >> 1)) * 28 * 128)
#define sHH (WS_HH + ((4 * aI + a4) * 112 + 14 * aK) * 1024)
#define sOut ((aI * 128 + a4 * 32) * 1024 + aD * 128)
#define sWd (aK * 224 * 1024 + aD * 128)
#define sS2 (WS_S2 + aI * 1024)
#define sX1 (WS_X1S + aI * 131072 + aA * 8192)
#define sHs (WS_HH + ((aI * 4 + nh) * 112 + 4 * aJ + mt) * 1024)
#define sFl (WS_F3 + (aI * 56 + aJ) * 128)
  u32 tra, rra, dvo, vf3, vsg, aT0, aT1, vres, ovo, ora, tvo, vfl, vsa;
  auto late_addresses = [&]() {
    u32 q = l;
    PIN(q);
    const u32 h = q >> 5, g = q >> 4, rq = (q >> 2) & 3, pc = q & 3, n = q & 15;
    tra = (h * 4 + rq) * 128 + (((g & 1) * 32 + pc * 8) ^ ((rq & 2) << 5));
    rra = L_RES + ((w >> 1) * 16 + g * 4 + rq) * 64 + (w & 1) * 32 + pc * 8;
    const u32 sw = ((q & 7) ^ ((g & 1) << 2)) << 4;
    tvo = (q >> 3) * 128 + sw;
    dvo = (q >> 3) * 1024 + sw;
    const u32 j0 = (14 * wk) >> 2, j1 = (14 * wk + 13) >> 2;
    vf3 = (j0 + q < j1 ? j0 + q : j1) * 128;
    vsg = nh * 256 + (q & 31) * 8;
    const u32 tta = (tra + h * 512) ^ (nh * 64);
    aT0 = L_T0 + tta; aT1 = L_T1 + tta;
    vres = (q >> 2) * 128 + pc * 16;
    ovo = ((w & 1) * 16 + n) * 1024 + ((w >> 1) * 4 + g) * 8;
    ora = ((w >> 1) * 4 + g) * 512 + ((w & 1) * 16 + n) * 16;
    vfl = (q & 31) * 32;
    vsa = L_V + (((q ^ g) & 1) ? 2112 : (q & 2) * 528) + h * 16;
    asm volatile("" : "+v"(tra), "+v"(rra), "+v"(dvo), "+v"(vf3), "+v"(vsg), "+v"(aT0), "+v"(aT1),
                 "+v"(vres), "+v"(ovo), "+v"(ora), "+v"(tvo), "+v"(vfl), "+v"(vsa));
  };
  auto stage_t = [&](u32 src, int c, u32 base) {
    const u32 so = src + ((c >> 1) * 16 + 2 * gI + (c & 1)) * 65536 + wa * 8192, m = base + wt * 8192;
    DMA4(rws, tvo, so, m, "sc1");
    DMA4(rws, tvo, so + 4096, m + 4096, "sc1");
  };
  f4 stp;
  auto col_sums = [&](int s0) {
    v4u a[16];
#pragma unroll
    for (int k = 0; k < 16; k++) a[k] = lds_ld<v4u>(vsa + (s0 + k) * 32);
    SB();
    stp = f4{0, 0, 0, 0};
#pragma unroll
    for (int k = 0; k < 16; k++) stp = mfma16(a[k], W[s0 + k], stp);
  };
  auto stage_res = [&](int half) { DMA(rws, vres, sRes + half * 16 * 65536, L_RES + w4 * 1024, "sc1"); };
  bool flagged = false;
  const int c0 = active ? 0 : (g4 < 2 ? g4 : 1);
  const u32 src0 = !active && g4 < 2 ? WS_X1T : WS_X1S;

  if (w < 4) {
    u32 arow = L_ROW + w * 2080 + l16, axg = L_ROW + (t4 & 3) * 2080 + (t4 >> 2) * 4;
    u32 amod = L_MODP + w * 256 + ((l & 7) * 8 + (l >> 5) * 4 + ((l >> 4) & 1) * 2) * 4;
    u32 vgx = (w * 64 + (l >> 2)) * 16384 + (l & 3) * 16, vgm = (l & 3) * 16 + (l & 4) * 4096;
    u32 ah = L_HR + (2046 - 512 * w - 2 * l) * 2 - 768, ax = L_XB + (512 * w + 2 * l) * 2;
    u32 ka, ha;
    {
      const int ch = w & 1, bh = w >> 1, m = l & 15, q = l >> 4;
      const int lag0 = 16 * (q >> 1) + m - 8 * (q & 1) - 7, sg = (m + 1) & 3;
      ka = L_KC + ch * 16640 + sg * 4160 + (lag0 - sg + 16) * 2 + bh * 256;
      ha = L_HR + ch * 4608 + 4080 - 3584 - 32 * m + 32 * (q >> 1) - 16 * (q & 1) + bh * 256;
    }
    u32 apw = L_P + w * 4096 + l16, kb = ka + 8;
    asm volatile("" : "+v"(arow), "+v"(axg), "+v"(amod), "+v"(vgx), "+v"(vgm), "+v"(ah), "+v"(ax), "+v"(v0));
    asm volatile("" : "+v"(ka), "+v"(kb), "+v"(ha), "+v"(apw), "+v"(wtr));
    take_tags();
    __builtin_amdgcn_s_setprio(2);
    if (ismod) VMW(5); else VMW(0);
    asm volatile("" : "+v"(row[0]), "+v"(row[1])::"memory");
    STAMP(1); Z(1, 1);
    {
      lds_st<v4u>(arow, row[0]);
      lds_st<v4u>(arow + 1040, row[1]);
      float t[2];
#pragma unroll
      for (int k = 0; k < 2; k++) {
        const float s = dot2(row[k].x, 0x3f803f80u, dot2(row[k].y, 0x3f803f80u, 0.f)) + dot2(row[k].z, 0x3f803f80u, dot2(row[k].w, 0x3f803f80u, 0.f));
        const float q = dot2(row[k].x, row[k].x, dot2(row[k].y, row[k].y, 0.f)) + dot2(row[k].z, row[k].z, dot2(row[k].w, row[k].w, 0.f));
        auto sw = __builtin_amdgcn_permlane32_swap(ubits(s), ubits(q), false, false);
        t[k] = fbits(sw[0]) + fbits(sw[1]);
      }
      Z(1, 2);
      lds_stv<u32>(M_XA + w * 4, tagA);
      if (Q0POS == 0 && active) stage_w(0);
      lds_wait4(l4, M_XA, tagA);
      Z(1, 3);
      const u32 d0 = lds_ld<u32>(axg), d1 = lds_ld<u32>(axg + 1040), d2 = lds_ld<u32>(axg + 256), d3 = lds_ld<u32>(axg + 256 + 1040);
      const u32 d4 = lds_ld<u32>(axg + 512), d5 = lds_ld<u32>(axg + 512 + 1040), d6 = lds_ld<u32>(axg + 768), d7 = lds_ld<u32>(axg + 768 + 1040);
      auto sw = __builtin_amdgcn_permlane16_swap(ubits(t[0]), ubits(t[1]), false, false);
      float u = fbits(sw[0]) + fbits(sw[1]);
      u += dppf<0xB1>(u); u += dppf<0x4E>(u); u += dppf<0x141>(u); u += dppf<0x140>(u);
      auto s2 = __builtin_amdgcn_permlane32_swap(ubits(u), ubits(u), false, false);
      const float mean = fbits(s2[0]) * (1.f / kD);
      const float var = rbf(fmaxf(fbits(s2[1]) * (1.f / kD) - mean * mean, 0.f));
      const u32 mr = pk(mean, __builtin_amdgcn_sqrtf(rbf(var + 1e-5f)));
      const u32 vo = t4 * 16, so = WS_XG + b * 16384;
      st128_dev(v4u{d0, tagA, d1, tagB}, rws, vo, so);
      st128_dev(v4u{d2, tagA, d3, tagB}, rws, vo, so + 4096);
      st128_dev(v4u{d4, tagA, d5, tagB}, rws, vo, so + 8192);
      st128_dev(v4u{d6, tagA, d7, tagB}, rws, vo, so + 12288);
      st64_dev(v2u{mr, (l & 16) ? tagB : tagA}, rws, (l & 47) == 0 ? (l >> 1) : kOOB, WS_STG + (b * 8 + 2 * w) * 8);
    }
    if (Q0POS == 1 && active) stage_w(0);
    STAMP(2); Z(1, 4);
    if (ismod) {
      if (Q0POS <= 1 && active) VMW(13); else VMW(5);
      asm volatile("" : "+v"(cv), "+v"(wm[0]), "+v"(wm[1]), "+v"(wm[2]), "+v"(wm[3])::"memory");
      const float c4[4] = {lo16(cv.x), hi16(cv.x), lo16(cv.y), hi16(cv.y)};
      float sl[4];
#pragma unroll
      for (int k = 0; k < 4; k++) sl[k] = c4[k] * __builtin_amdgcn_rcpf(1.f + __builtin_amdgcn_exp2f(c4[k] * -kLog2e));
      const u32 s01 = pk(sl[0], sl[1]), s23 = pk(sl[2], sl[3]);
      float a[8];
#pragma unroll
      for (int e = 0; e < 4; e++) {
        a[2 * e] = dot2(__builtin_amdgcn_perm(wm[1][e], wm[0][e], 0x05040100u), s01, dot2(__builtin_amdgcn_perm(wm[3][e], wm[2][e], 0x05040100u), s23, 0.f));
        a[2 * e + 1] = dot2(__builtin_amdgcn_perm(wm[1][e], wm[0][e], 0x07060302u), s01, dot2(__builtin_amdgcn_perm(wm[3][e], wm[2][e], 0x07060302u), s23, 0.f));
      }
      float t[4], u[2];
#pragma unroll
      for (int e = 0; e < 4; e++) {
        auto s = __builtin_amdgcn_permlane32_swap(ubits(a[e]), ubits(a[e + 4]), false, false);
        t[e] = fbits(s[0]) + fbits(s[1]);
      }
#pragma unroll
      for (int e = 0; e < 2; e++) {
        auto s = __builtin_amdgcn_permlane16_swap(ubits(t[e]), ubits(t[e + 2]), false, false);
        u[e] = fbits(s[0]) + fbits(s[1]);
        u[e] += dppf<0x128>(u[e]);
      }
      lds_st<f2>(amod, f2{u[0], u[1]});
      lds_stv<u32>(M_XA + 16 + w * 4, tagB);
    }
    if (ismod && w == 0) {
      lds_wait4(l4, M_XA + 16, tagB);
      f2 t[4];
#pragma unroll
      for (int k = 0; k < 4; k++) t[k] = lds_ld<f2>(L_MODP + k * 256 + (l & 31) * 8);
      const f2 s = (t[0] + t[1]) + (t[2] + t[3]);
      st128_dev(v4u{ubits(s.x), tagA, ubits(s.y), tagB}, rws, l < 32 ? l * 64 : kOOB, WS_MODG + mgrp * 2048 + mkq * 16);
    }
    STAMP(3); Z(1, 5);
    if (Q0POS == 2 && active) stage_w(0);
    STAMP(4); Z(1, 8);
    v4u gx[4], gs[4], gm;
#define GATHER_S()                                                        \
  LD128(gm, rws, vgm, WS_MODG + cp * 64, "sc1");                          \
  LD128(gs[0], rws, l16, WS_STG + w * 4096, "sc1");                       \
  LD128(gs[1], rws, l16, WS_STG + w * 4096, "offset:1024 sc1");           \
  LD128(gs[2], rws, l16, WS_STG + w * 4096, "offset:2048 sc1");           \
  LD128(gs[3], rws, l16, WS_STG + w * 4096, "offset:3072 sc1")
#define GATHER_X() _Pragma("unroll") for (int k = 0; k < 4; k++) LD128(gx[k], rws, vgx, WS_XG + cp * 64 + k * 16 * 16384, "sc1")
#define GATHER_WAIT() VMW(0); GATHER_PIN()
#define GATHER_PIN() \
  do { asm volatile("" : "+v"(gx[0]), "+v"(gx[1]), "+v"(gx[2]), "+v"(gx[3]), "+v"(gs[0]), "+v"(gs[1]), "+v"(gs[2]), "+v"(gs[3]), "+v"(gm)::"memory"); } while (0)
    late_addresses();
    lds_wait4(l4, M_XA + GSYNC, tagA);
    if (GDELAY) __builtin_amdgcn_s_sleep(GDELAY);
    const bool summer = ismod && mkq == 0 && mgrp >= 24 && w == 0;
    v4u gq[2];
    const u32 vgq = (l >> 2) * 64 + (l & 3) * 16;
#define GATHER_Q() LD128(gq[0], rws, vgq, WS_MODG + mgrp * 2048, "sc1"); LD128(gq[1], rws, vgq, WS_MODG + mgrp * 2048, "offset:1024 sc1")
    if (summer) { GATHER_Q(); }
    GATHER_S();
    GATHER_X();
    STAMP(5); Z(1, 10);
    VMW(0);
    if (summer) {
      asm volatile("" : "+v"(gq[0]), "+v"(gq[1])::"memory");
      RETRY(any_set(tag_bad(gq[0], tagA, tagB, tag_bad(gq[1], tagA, tagB))), GATHER_Q(); VMW(0); asm volatile("" : "+v"(gq[0]), "+v"(gq[1])::"memory");)
      float z[4] = {fbits(gq[0].x), fbits(gq[0].z), fbits(gq[1].x), fbits(gq[1].z)};
#pragma unroll
      for (int k = 0; k < 4; k++) { z[k] += dppf<0xB1>(z[k]); z[k] += dppf<0x4E>(z[k]); }
      const u32 u = (l >> 2) + (l & 1) * 16;
      st64_dev(v2u{(l & 1) ? pk(z[2], z[3]) : pk(z[0], z[1]), (u & 1) ? tagB : tagA}, rws, (l & 2) ? kOOB : u * 8, WS_MODF + (mgrp - 24) * 256);
    }
    GATHER_PIN();
    STAMP(6); Z(1, 11);
    auto bad_s = [&]() { return tag_bad(gm, tagA, tagB, tag_bad(gs[0], tagA, tagB, tag_bad(gs[1], tagA, tagB, tag_bad(gs[2], tagA, tagB, tag_bad(gs[3], tagA, tagB))))); };
    auto bad_x = [&]() { return tag_bad(gx[0], tagA, tagB, tag_bad(gx[1], tagA, tagB, tag_bad(gx[2], tagA, tagB, tag_bad(gx[3], tagA, tagB)))); };
    if (any_set(bad_s() | bad_x())) {
      RETRY(any_set(bad_s()), GATHER_S(); GATHER_WAIT();)
      RETRY(any_set(bad_x()), GATHER_X(); GATHER_WAIT();)
    }
    lds_wait4(l4, M_XA + 96, tagA);
    STAMP(7); Z(1, 12);
    float m0 = fbits(gm.x), m1 = fbits(gm.z);
    m0 += dppf<0xB1>(m0); m1 += dppf<0xB1>(m1);
    m0 += dppf<0x4E>(m0); m1 += dppf<0x4E>(m1);
    const u32 mm = pk(m0, m1), m1p = pk2(up2(mm) + 1.f);
    const f2 sa = up2(__builtin_amdgcn_readlane(mm, 0)), s1 = up2(__builtin_amdgcn_readlane(m1p, 4));
#pragma unroll
    for (int k = 0; k < 4; k++) {
      u32 h[2];
#pragma unroll
      for (int e = 0; e < 2; e++) {
        const float rd = __builtin_amdgcn_rcpf(hi16(gs[k][2 * e])), mr = lo16(gs[k][2 * e]) * -rd;
        h[e] = pk2(fma2(fma2(up2(gx[k][2 * e]), f2{rd, rd}, f2{mr, mr}), s1, sa));
      }
      lds_st<u32>(ah + 768 - k * 256, __builtin_amdgcn_perm(h[0], h[1], 0x05040100u));
      lds_st<u32>(ah + 768 - k * 256 + 4608, __builtin_amdgcn_perm(h[0], h[1], 0x07060302u));
      lds_st<u32>(ax + k * 256, __builtin_amdgcn_perm(gx[k].z, gx[k].x, 0x05040100u));
      lds_st<u32>(ax + k * 256 + 4096, __builtin_amdgcn_perm(gx[k].z, gx[k].x, 0x07060302u));
    }
    Z(1, 13);
    BAR();
    STAMP(8); Z(6, 1); Z(1, 14);

    v2u A[4][8][2];
    v4u B[4][8];
    f4 acc[4];
    auto rdA = [&](int bb, int a, int hf) { A[bb][a][hf] = lds_ldv<v2u>((hf ? kb : ka) + 512 * a + 64 * bb); };
    auto rdB = [&](int bb, int i) { B[bb][i] = lds_ldv<v4u>(ha + 512 * (7 - i) + 64 * bb); };
    auto cmm = [&](int bb, int a, int i, int t) { acc[t] = mfma16(v4u{A[bb][a][0].x, A[bb][a][0].y, A[bb][a][1].x, A[bb][a][1].y}, B[bb][i], acc[t]); };
    auto rd1 = [&](int bb, int r) {
      if (bb < 4) {
        if (r < 2) rdA(bb, 0, r); else if (r < 6) rdB(bb, r - 2); else rdA(bb, (r - 4) >> 1, r & 1);
      } else {
        if (r < 2) rdA(0, 0, r); else if (r < 7) rdB(0, 9 - r); else if (r < 11) rdA(0, (r - 5) >> 1, (r - 1) & 1);
      }
    };
#pragma unroll
    for (int r = 0; r < 12; r++) rd1(0, r);
#pragma unroll
    for (int i = 0; i < 4; i++) acc[i] = f4{0, 0, 0, 0};
#pragma unroll
    for (int bb = 0; bb < 4; bb++) {
      int r = 0;
#pragma unroll
      for (int a = 0; a < 4; a++)
#pragma unroll
        for (int i = a; i < 4; i++) {
          SB();
          cmm(bb, a, i - a, i);
          SB();
          rd1(bb + 1, r++);
          if (r <= 3) rd1(bb + 1, r++);
        }
    }
    SB();
#pragma unroll
    for (int i = 0; i < 4; i++) lds_st<f4>(apw + i * 1024, acc[i]);
    READ_W(0);
    Z(6, 2);
    Z(6, 3);
    BAR();
    __builtin_amdgcn_s_setprio(0);
    STAMP(9); Z(6, 4); Z(7, 1); ZC(1);
    READ_W(1);
    lds_stv<u32>(M_XA + 48 + w * 4, tagA);
    Z(6, 5);
#pragma unroll
    for (int i = 0; i < 4; i++) acc[i] = f4{0, 0, 0, 0};
#pragma unroll
    for (int s = 0; s < 32; s++) {
      const int bb = s >> 3, a = s & 7, n = s + 3, i0 = a > 4 ? a : 4;
      auto rd2 = [&](int r) {
        if (r < 2) { if (n < 32) rdA(n >> 3, n & 7, r); }
        else if (r == 2) { if (a < 3) rdB(bb, 2 - a); else if (a < 7 && bb < 3) rdB(bb + 1, 9 - a); }
        else if (r == 3) { if (a == 2 && bb < 3) rdB(bb + 1, 7); }
      };
#pragma unroll
      for (int i = i0; i < 8; i++) {
        SB();
        cmm(bb, a, i - a, i - 4);
        SB();
        rd2(i - i0);
      }
#pragma unroll
      for (int r = 8 - i0; r < 4; r++) rd2(r);
    }
    SB();
    STAMP(10); Z(6, 9);
    lds_wait4(l4, M_XA + 112, tagA);
#pragma unroll
    for (int i = 0; i < 4; i++) lds_st<f4>(apw + i * 1024, acc[i]);
    lds_stv<u32>(M_XA + 128 + w * 4, tagA);
    Z(7, 4);
    POLL_X1(0);
    lds_wait4(l4, M_XA + 128, tagA);
    STAMP(11); Z(6, 10); Z(7, 5);
    VMW(0);
    Z(7, 6);
    RETRY_X1(0)
    Z(7, 7);
    stage_t(src0, c0, L_T0);
    Z(7, 8);
    lds_wait4(l4, M_XA + 144, tagA);
    READ_W(2);
    READ_W(3);
  } else {
    const int ch = w4 >> 1, mode = (w4 & 1) * 64 + l;
    const u32 ldt2 = ((const CAS u32*)p.logdt)[cp];
    u32 kix[3][7];
    {
      const u32 gb = L_G + ch * 1352 + 8, gc = gb + 512 - 2 * 0x4380;
      const int l0 = 4 * (t4 & 127);
      const float f0 = (float)l0;
#pragma unroll
      for (int k = 0; k < 3; k++)
#pragma unroll
        for (int e = 0; e < 7; e += 2) {
          float x0 = f0 + (512.f * k + e), x1 = f0 + (512.f * k + e + 1);
          asm volatile("" : "+v"(x0), "+v"(x1));
          const u32 q = pk(x0, e < 6 ? x1 : x0);
          asm("v_mad_u32_u16 %0, %1, 2, %2" : "=v"(kix[k][e]) : "v"(q), "v"(gc));
          if (e < 6) asm("v_mad_u32_u16 %0, %1, 2, %2 op_sel:[1,0,0,0]" : "=v"(kix[k][e + 1]) : "v"(q), "v"(gc));
        }
      if ((w4 & 1) == 0) {
#pragma unroll
        for (int e = 0; e < 7; e++) kix[0][e] = l0 + e < 256 ? gb + 2 * (l0 + e) : kix[0][e];
      }
#pragma unroll
      for (int k = 0; k < 3; k++)
        asm volatile("" : "+v"(kix[k][0]), "+v"(kix[k][1]), "+v"(kix[k][2]), "+v"(kix[k][3]), "+v"(kix[k][4]), "+v"(kix[k][5]), "+v"(kix[k][6]));
    }
    lds_st<v4u>(M_CNT, v4u{0u, 0u, 0u, 0u});
    float dt = rbf(exp_acc(ch ? hi16(ldt2) : lo16(ldt2)));
    u32 fb = (L_FACT + ch * 32768 + mode * 128) | ((mode >> 1) & 1) << 6 | ((mode >> 2) & 1) << 5 | (mode & 1) << 4;
    const int q = l >> 4, rq = (l >> 2) & 3, pp = l & 3, nt = w4 & 1;
    const u32 km = 4 * q + (rq >> 1);
    u32 kb = (L_FACT + ch * 32768 + km * 128 + (pp & 1) * 8) | (((rq & 1) * 64 + (pp >> 1) * 16) ^ (((km >> 2) & 1) << 5 | (km & 1) << 4));
    const u32 bp = 16 * nt + (l & 15), gt = L_G + ch * 1352 + 8;
    u32 ga0 = gt + (q < 2 ? 8 * bp + 4 * q : (q == 2 ? 256 + 4 * bp : 384 + 2 * bp)) * 2, ga1 = ga0 + (q < 3 ? 4 : 128);
    u32 gb0 = gt + (q == 0 ? 512 + bp : 640 + bp) * 2, gbs = q == 0 ? 64 : 0;
    u32 akc = L_KC + ch * 16640 + ((t4 & 127) + 4) * 8;
    const int et0 = 256 * ((t4 >> 5) & 3) + 16 * ((t4 >> 1) & 15) + 8 * (t4 & 1);
    u32 epa = L_P + ch * 4096 + ((t4 >> 5) & 3) * 1024 + (((t4 >> 1) & 15) + 32 * (t4 & 1)) * 16;
    u32 eha = L_HR + ch * 4608 + (1016 - et0) * 2, exa = L_XB + ch * 4096 + et0 * 2, evo = (et0 >> 6) * 65536 + (et0 & 63) * 2;
    const u32 dsk2 = ((const CAS u32*)p.dskip)[cp];
    asm volatile("" : "+v"(epa), "+v"(eha), "+v"(exa), "+v"(evo), "+v"(wtr));
    asm volatile("" : "+v"(dt), "+v"(fb), "+v"(kb), "+v"(ga0), "+v"(ga1), "+v"(gb0), "+v"(gbs), "+v"(akc), "+v"(v0));
    take_tags();
    VMW(0);
    do { asm volatile("" : "+v"(pAr), "+v"(pAi), "+v"(pBr), "+v"(pBi), "+v"(pCr), "+v"(pCi)::"memory"); } while (0);
    STAMP(1); Z(1, 1);
    const float al = (dt * lo16(pAr)) * kLog2e, be = (dt * lo16(pAi)) * 0.159154943091895336f;
    const f2 e1 = cexpv(al, be, 1.f), e64 = cexpv(al, be, 64.f);
    const f2 e2 = cmul(e1, e1), e128 = cmul(e64, e64), e4 = cmul(e2, e2), e256 = cmul(e128, e128);
    {
      const f2 e8 = cmul(e4, e4);
      f2 v[4][8];
      v[0][0] = f2{1.f, 0.f}; v[0][1] = e8;
      v[1][0] = e64; v[2][0] = e128; v[3][0] = cmul(e128, e64);
#pragma unroll
      for (int i = 1; i < 8; i++)
#pragma unroll
        for (int c = 0; c < 4; c++)
          if (c + i > 1) v[c][i] = cmul(v[c][i - 1], e8);
#pragma unroll
      for (int c = 0; c < 4; c++) {
        lds_st<v4u>(fb ^ (c << 4), v4u{pkh(v[c][0].x, v[c][1].x), pkh(v[c][2].x, v[c][3].x), pkh(v[c][4].x, v[c][5].x), pkh(v[c][6].x, v[c][7].x)});
        lds_st<v4u>(fb ^ ((4 + c) << 4), v4u{pkh(v[c][0].y, v[c][1].y), pkh(v[c][2].y, v[c][3].y), pkh(v[c][4].y, v[c][5].y), pkh(v[c][6].y, v[c][7].y)});
      }
    }
    STAMP(2); Z(1, 4);
    {
      f2 wv = cmul(f2{lo16(pCr), lo16(pCi)}, f2{lo16(pBr), lo16(pBi)} * dt);
      u32 ex = wave_umax(umax(ubits(wv.x) & 0x7fffffffu, ubits(wv.y) & 0x7fffffffu)) >> 23;
      ex = ex < 14 ? 14 : (ex > 254 ? 254 : ex);
      wv *= fbits((267 - ex) << 23);
      lds_st<u32>(M_INV + w4 * 4, (ex - 13) << 23);
      f2 v[24];
      v[0] = wv; v[8] = cmul(wv, e256);
      v[1] = cmul(wv, e1); v[2] = cmul(wv, e2); v[4] = cmul(wv, e4);
      v[12] = cmul(v[8], e256);
      v[3] = cmul(v[1], e2); v[5] = cmul(v[1], e4); v[6] = cmul(v[2], e4);
      v[14] = cmul(v[12], e256);
      v[7] = cmul(v[3], e4); v[9] = cmul(v[8], e2); v[10] = cmul(v[8], e4);
      v[16] = cmul(v[14], e256);
      v[11] = cmul(v[9], e4); v[13] = cmul(v[12], e4);
      v[17] = cmul(v[16], e256);
      v[15] = cmul(v[14], e4);
      v[18] = cmul(v[17], e256); v[19] = cmul(v[18], e256); v[20] = cmul(v[19], e256);
      v[21] = v[22] = v[23] = f2{0.f, 0.f};
#pragma unroll
      for (int c = 0; c < 3; c++) {
        const f2* z = v + 8 * c;
        lds_st<v4u>((fb + 16384) ^ (c << 4), v4u{pkh(z[0].x, z[1].x), pkh(z[2].x, z[3].x), pkh(z[4].x, z[5].x), pkh(z[6].x, z[7].x)});
        lds_st<v4u>((fb + 16384) ^ ((4 + c) << 4), v4u{pkh(-z[0].y, -z[1].y), pkh(-z[2].y, -z[3].y), pkh(-z[4].y, -z[5].y), pkh(-z[6].y, -z[7].y)});
      }
    }
    STAMP(3); Z(1, 5);
    lds_stv<u32>(M_XA + 80 + w4 * 4, tagA);
    lds_wait4(l4, M_XA + 80, tagA);
    Z(1, 6);
    {
      const u32 ab = kb ^ (nt * 32), aa = kb + 16384;
      v4u A0[8], A1[8], B[8];
#pragma unroll
      for (int ks = 0; ks < 8; ks++) {
        B[ks] = tr_frag(ab + ks * 2048, (ab ^ 64) + ks * 2048 + 256);
        A0[ks] = tr_frag(aa + ks * 2048, (aa ^ 64) + ks * 2048 + 256);
        A1[ks] = tr_frag((aa ^ 32) + ks * 2048, (aa ^ 96) + ks * 2048 + 256);
        SB();
      }
      const f2 inv = lds_ld<f2>(M_INV + ch * 8);
      Z(1, 7);
      SB();
      f4 a0[2] = {f4{0, 0, 0, 0}, f4{0, 0, 0, 0}}, a1[2] = {f4{0, 0, 0, 0}, f4{0, 0, 0, 0}};
#pragma unroll
      for (int ks = 0; ks < 8; ks++) {
        a0[ks >> 2] = mfma16h(A0[ks], B[ks], a0[ks >> 2]);
        a1[ks >> 2] = mfma16h(A1[ks], B[ks], a1[ks >> 2]);
      }
      const f4 k0 = a0[0] * inv.x + a0[1] * inv.y, k1 = a1[0] * inv.x + a1[1] * inv.y;
      lds_st<u32>(ga0, pk(k0.x, k0.y));
      lds_st<u32>(ga1, pk(k0.z, k0.w));
      if (q < 2) {
        const u32 p01 = pk(k1.x, k1.y), p23 = pk(k1.z, k1.w);
        lds_st<u16>(gb0 + 3 * gbs, (u16)(p23 >> 16));
        lds_st<u16>(gb0 + 2 * gbs, (u16)p23);
        lds_st<u16>(gb0 + gbs, (u16)(p01 >> 16));
        lds_st<u16>(gb0, (u16)p01);
      }
    }
    STAMP(4); Z(1, 8);
    lds_stv<u32>(M_XA + 96 + w4 * 4, tagA);
    LD128(g9, rws, vg9, WS_MODG, "sc1");
    lds_wait4(l4, M_XA + 96, tagA);
    Z(1, 9);
    {
      u32 v[4][7];
#pragma unroll
      for (int k = 0; k < 4; k++)
#pragma unroll
        for (int e = 0; e < 7; e++) v[k][e] = lds_ld<u16>(kix[k < 3 ? k : 2][e] + (k == 3 ? 128 : 0));
      const u32 s = (l >> 2) & 3, c = (l >> 4) & 1;
      const u64 q0 = lds_ld<u64>(L_G + c * 1352 + 8);
      SB();
#pragma unroll
      for (int k = 0; k < 4; k++) {
        u32 z[6];
#pragma unroll
        for (int e = 0; e < 6; e++) z[e] = v[k][e] | v[k][e + 1] << 16;
#pragma unroll
        for (int x = 0; x < 4; x++) lds_st<v2u>(akc + k * 1024 + x * 4160, v2u{z[x], z[x + 2]});
      }
      lds_st<u64>(L_KC + c * 16640 + s * 4160 + (l & 3) * 8, ((l & 3) == 3 && s) ? q0 << ((64 - 16 * s) & 63) : 0ull);
      lds_st<u32>(L_HR + ch * 4608 + 4096 + (t4 & 127) * 4, 0u);
    }
    __builtin_amdgcn_s_setprio(0);
    STAMP(5); Z(1, 10);
    late_addresses();
    STAMP(6); Z(1, 11);
    Z(1, 13);
    BAR();
    STAMP(8); Z(6, 1); Z(1, 14);

    __builtin_amdgcn_s_setprio(3);
    if (active) stage_w(1);
    READ_W(0);
    VMW(0);
    PIN(g9);
    Z(6, 2);
    u32 mga, mcb;
    RETRY(any_set(tag_bad(g9, tagA, tagB)), LD128(g9, rws, vg9, WS_MODG, "sc1"); VMW(0); PIN(g9);)
    {
      float z0 = fbits(g9.x), z1 = fbits(g9.z);
      z0 += dppf<0xB1>(z0); z1 += dppf<0xB1>(z1);
      z0 += dppf<0x4E>(z0); z1 += dppf<0x4E>(z1);
      asm volatile("" : "+v"(z0), "+v"(z1));
      const u32 z = pk(z0, z1);
      mga = __builtin_amdgcn_readlane(z, 0);
      mcb = __builtin_amdgcn_readlane(z, 4);
    }
    const float gav = ch ? hi16(mga) : lo16(mga), dsk = ch ? hi16(dsk2) : lo16(dsk2), scb = rbf(1.f + (ch ? hi16(mcb) : lo16(mcb)));
    auto conv_epilogue = [&](int half) {
      const f4 a0 = lds_ld<f4>(epa), a1 = lds_ld<f4>(epa + 8192), b0 = lds_ld<f4>(epa + 256), b1 = lds_ld<f4>(epa + 8192 + 256);
      const v4u hv = lds_ld<v4u>(eha + (1 - half) * 2048), xv = lds_ld<v4u>(exa + half * 2048);
      lds_stv<u32>(M_XA + 112 + w4 * 4, tagA + half);
      float z[8];
#pragma unroll
      for (int e = 0; e < 8; e++) {
        const float y = e < 4 ? a0[e & 3] + a1[e & 3] : b0[e & 3] + b1[e & 3];
        const float hh = (e & 1) ? lo16(hv[3 - (e >> 1)]) : hi16(hv[3 - (e >> 1)]), xx = (e & 1) ? hi16(xv[e >> 1]) : lo16(xv[e >> 1]);
        z[e] = __builtin_fmaf(gav, __builtin_fmaf(hh, dsk, y), xx);
      }
      asm volatile("" : "+v"(z[0]), "+v"(z[1]), "+v"(z[2]), "+v"(z[3]), "+v"(z[4]), "+v"(z[5]), "+v"(z[6]), "+v"(z[7]));
      float y[8];
#pragma unroll
      for (int e = 0; e < 8; e++) y[e] = z[e] * scb;
      asm volatile("" : "+v"(y[0]), "+v"(y[1]), "+v"(y[2]), "+v"(y[3]), "+v"(y[4]), "+v"(y[5]), "+v"(y[6]), "+v"(y[7]));
      const u32 so = half * 16 * 65536 + (2 * cp + ch) * 128;
      st128_dev(v4u{pk(y[0], y[1]), pk(y[2], y[3]), pk(y[4], y[5]), pk(y[6], y[7])}, rws, evo, WS_X1S + so);
      st128_dev(v4u{pk(z[0], z[1]), pk(z[2], z[3]), pk(z[4], z[5]), pk(z[6], z[7])}, rws, evo, WS_X1T + so);
    };
    Z(6, 3);
    BAR();
    STAMP(9); Z(6, 4); Z(7, 1); ZC(1);
    conv_epilogue(0);
    Z(6, 5);
    Z(6, 6);
    READ_W(1);
    lds_stv<u32>(M_XA + 64 + w4 * 4, tagA);
    VMW(0);
    Z(6, 7);
    Z(7, 2);
    if (lds_ticket(M_CNT) == 3) flag_x1(0);
    Z(7, 3);
    GATHER2();
    if (active) {
      stage_w(2);
      lds_wait4x2(l4, M_XA + 48, M_XA + 64, tagA);
      stage_w(3);
      VMW(16);
    } else {
      const rsrc_t rd = mk(p.wd, kF * kD * 2);
      const u32 vt = l < 56 ? l * 128 : kOOB, st = (((r >> 3) * 8 + xcd) * 448 + w4 * 112) * 128;
      if (WTOUCH) { TOUCH(rd, vt, st); TOUCH(rd, vt, st + 56 * 128); VMW(2); } else VMW(0);
    }
    Z(6, 8);
    {
      const u32 tg = (l & 1) ? tagB : tagA;
      GATHER2_PIN();
      RETRY(any_set(xo(gf[0].y, tg, xo(gf[1].y, tg, gf[2].y ^ tg))), GATHER2(); VMW(0); GATHER2_PIN();)
      float s0 = lo16(gf[1].x) + 1.f, s1 = hi16(gf[1].x) + 1.f;
      asm volatile("" : "+v"(s0), "+v"(s1));
      const u32 av = L_V + ((t4 - 64 * kr) & 255) * 4;
      lds_st<u32>(av, pk(s0, s1));
      lds_st<u32>(av + 1056, gf[0].x);
      lds_st<u32>(av + 2112, 0u);
      lds_st<f2>(L_GB + (l & 31) * 8, up2(gf[2].x));
    }
    __builtin_amdgcn_s_setprio(0);
    STAMP(10); Z(6, 9);
    lds_wait4(l4, M_XA + 128, tagA);
    Z(7, 4);
    POLL_X1(0);
    conv_epilogue(1);
    STAMP(11); Z(6, 10); Z(7, 5);
    VMW(2);
    Z(7, 6);
    lds_stv<u32>(M_XA + 144 + w4 * 4, tagA);
    RETRY_X1(0)
    Z(7, 7);
    lds_wait4(l4, M_XA + 112, tagA + 1);
    stage_t(src0, c0, L_T0);
    Z(7, 8);
    VMW(8);
    flagged = lds_ticket(M_CNT + 12) == 3;
    if (flagged) flag_x1(1);
    lds_wait4(l4, M_XA + 144, tagA);
    READ_W(2);
    READ_W(3);
  }
  STAMP(12); Z(6, 12); Z(7, 9);
  PINS(tagA); PINS(tagB);
  const rsrc_t rwd = mk(p.wd, kF * kD * 2);
#define STAGE_D(s0, s1, base)                                                                  \
  _Pragma("unroll") for (int s_ = (s0); s_ < (s1); s_++)                                       \
      asm volatile("s_mov_b32 m0, %3\n\ts_nop 0\n\tbuffer_load_dwordx4 %0, %1, %2 offen lds\n\t" \
                   "buffer_load_dwordx4 %0, %1, %4 offen offset:1024 lds" ::"v"(dvo), "s"(rwd), "s"(WD_OFF(s_)),      \
                   "s"((base) + (s_ - (s0)) * 2048), "s"(WD_OFF(s_) + 7168) : "memory")
  auto late_scalars = [&]() {
    aR = aR >= 7 ? aR - 7 : aR;
    PINS(aI); PINS(a4); PINS(aD); PINS(aJ); PINS(aK); PINS(aA); PINS(aR);
  };
  auto po = [&](int k) { return (aR + k >= 7 ? aR + k - 7 : aR + k) * 2048; };
#define WD_OFF(s) (sWd + po((s) >> 1) * 16 + ((s) & 1) * 16384)
  f16v acc[2];
  v2u resa = {0u, 0u}, resb = {0u, 0u};
  v4u Wd[14][2], hf[14];
#define READ_D(s0, s1, base)                                                                  \
  _Pragma("unroll") for (int s = (s0); s < (s1); s++) _Pragma("unroll") for (int m2 = 0; m2 < 2; m2++) \
      Wd[s][m2] = tr_frag((base) + (tra ^ (m2 * 64)), (base) + (tra ^ (m2 * 64)), (s - (s0)) * 2048)
#define READ_D1() READ_D(3, 7, L_T0 + w * 8192); READ_D(7, 8, L_D0 + w * 2048)
  v2u fa;
#define POLL_HH(f, half) LD64(f, rws, vf3, sF3 + (half) * 16 * 28 * 128, "sc1")
#define RETRY_HH(half) RETRY(any_set(tag_bad(fa, tagA, tagB)), __builtin_amdgcn_s_sleep(2); POLL_HH(fa, half); VMW(0); PIN(fa);)
  auto wait_hh = [&](int half) { POLL_HH(fa, half); VMW(0); PIN(fa); RETRY_HH(half) };
#define LDH2(s, so)                                                                                                    \
  asm volatile("buffer_load_dwordx4 %0, %2, %3, %4 offen sc1\n\tbuffer_load_dwordx4 %1, %2, %3, %4 offen offset:1024 sc1" \
               : "=&v"(hf[s]), "=&v"(hf[s + 1])                                                                        \
               : "v"(l16), "s"(rws), "s"((so) + po((s) >> 1))                                                          \
               : "memory")
  auto hh_off = [&](int half) { return sHH + half * 32 * 112 * 1024; };

  Z(6, 13);
  Z(6, 14);
  if (active) {
    if (nh) col_sums(16); else col_sums(0);
    if (l < 16) lds_st<f4>(L_ST + w * 256 + l16, stp);
  }
  Z(6, 15);
  late_scalars();
  Z(6, 16);
  if (active) {
    STAMP(13); Z(6, 17); Z(7, 10);
    if (flagged) VMW(1); else VMW(0);
    Z(7, 11);
    BAR();
    STAMP(14); Z(2, 20); Z(3, 20); Z(6, 18); Z(7, 12); Z(8, 21); ZC(2);
    if (b == 0 && w == 4) st64_dev(v2u{tagA, tagB}, rws, v0, WS_NONCE);
    v4u stf;
    auto take_sums = [&]() {
      const u32 a = L_ST + mt * 256 + (l & 15) * 16 + ((l >> 4) & 1) * 4;
      float x = lds_ld<float>(a) + lds_ld<float>(a + 1024), y = lds_ld<float>(a + 8) + lds_ld<float>(a + 1024 + 8);
      asm volatile("" : "+v"(x), "+v"(y));
      const u32 hi = pk(x, y);
      x -= lo16(hi); y -= hi16(hi);
      asm volatile("" : "+v"(x), "+v"(y));
      const u32 lo = pk(x, y), m = l < 32 ? ~0u : 0u;
      stf = v4u{__builtin_amdgcn_perm(lo, hi, 0x05040100u) & m, __builtin_amdgcn_perm(lo, hi, 0x07060302u) & m, 0u, 0u};
      PIN(stf);
    };
    float den, erg, ez[2], eo[8];
    v2u sg[4];
    v4u bf;
    const u32 mytag = (l & 1) ? tagB : tagA;
#define TBLK(c) (((c) >> 1) * 16 + ((c) & 1))
#define LD_STAT(c) LD64(sg[c], rws, vsg, sS2 + TBLK(c) * 512, "sc1")
    auto stat_check = [&](int c) {
      PIN(sg[c]);
      RETRY(any_set(sg[c].y ^ mytag), LD_STAT(c); VMW(0); PIN(sg[c]);)
    };
    auto stat_take = [&](int c) {
      den = hi16(sg[c].x);
      erg = __builtin_amdgcn_rcpf(den) * -kLog2e;
      bf = v4u{__builtin_amdgcn_perm(sg[c].x, sg[c].x, 0x01000100u) ^ 0x80008000u, __builtin_amdgcn_perm(sg[c].x, sg[c].x, 0x03020302u), 0u, 0u};
      asm volatile("" : "+v"(den), "+v"(erg), "+v"(bf));
    };
    auto stat_fold = [&](int c) { acc[c & 1] = mfma32(stf, bf, acc[c & 1]); };
    auto epi_a = [&](const f16v& ac, int k) {
      ez[k & 1] = __builtin_amdgcn_exp2f(ac[8 * (k >> 2) + (k & 3)] * erg);
      PIN(ez[k & 1]);
    };
    auto epi_b = [&](const f16v& ac, int k) {
      eo[k] = ac[8 * (k >> 2) + 4 + (k & 3)] * __builtin_amdgcn_rcpf(__builtin_fmaf(ez[k & 1], den, den));
      PIN(eo[k]);
    };
    u32 nX1 = sX1 + 65536, nX2 = nX1 ^ 32768, nHs = 0, cX1, cX2, cHs;
    const u32 vflb = ((aA * 32 + (l >> 5) * 128 + (l & 31)) & 255) * 32;
#define POLL_X1B() LD64(fx, rws, vflb, WS_F2 + 65536 + xcd * 8192, "sc1")
    auto epi_store = [&](int c) {
      st128_dev(v4u{pk(eo[0], eo[1]), pk(eo[2], eo[3]), pk(eo[4], eo[5]), pk(eo[6], eo[7])}, rws, l16, c < 3 ? cHs : sHs + TBLK(3) * 2 * 112 * 1024);
    };
    auto flag_hh = [&](int c) { st64_dev(v2u{tagA, tagB}, rws, v0, sFl + TBLK(c) * 28 * 128); };
#define DMA_M0(m) asm volatile("s_mov_b32 m0, %0" ::"s"(m) : "memory")
#define DMA_K(rs, vo, so, off, mods) \
  asm volatile("buffer_load_dwordx4 %0, %1, %2 offen offset:%3 " mods " lds" ::"v"(vo), "s"(rs), "s"(so), "i"(off) : "memory")
#pragma unroll
    for (int c = 0; c < 4; c++) {
      const u32 a0 = (c & 1) ? aT1 : aT0, tn = (c & 1) ? L_T0 : L_T1;
      v4u F[32];
#pragma unroll
      for (int s = 0; s < 4; s++) F[s] = tr_frag(a0 + s * 2048, a0 + s * 2048 + 512);
      cX1 = nX1; cX2 = nX2; cHs = nHs;
      f16v ac = f16v{};
#pragma unroll
      for (int s = 0; s < 32; s++) {
        if (!(s & 1)) { asm volatile("" ::"v"(F[s]), "v"(F[s + 1])); SB(); }
        ac = mfma32(W[s], F[s], ac);
        SB();
        if (s + 4 < 32) F[s + 4] = tr_frag(a0 + (s + 4) * 2048, a0 + (s + 4) * 2048 + 512);
        if (c == 1) {
          if (s == 0) LD_STAT(0);
          if (s == 1 && nh == 0) { VMW(3); PIN(fx); RETRY(any_set(tag_bad(fx, tagA, tagB)), __builtin_amdgcn_s_sleep(PSLEEP); POLL_X1B(); VMW(0); PIN(fx);) }
          if (s == 9) { if (nh) VMW(0); else VMW(7); }
        }
        if (c > 0) {
          const int t0 = c == 1 ? 9 : 0;
          if (s == t0) stat_check(c - 1);
          if (s == t0 + 1) stat_take(c - 1);
          if (s == t0 + 2) stat_fold(c - 1);
          if (s == 18) LD_STAT(c);
        }
        if (c >= 2) {
          if (s == 1) { VMW(2); lds_stv<u32>(M_XA + 192 + w * 4, tagA + c); }
          if (s == 10 && w == 4) { lds_wait4x2(l4, M_XA + 192, M_XA + 208, tagA + c); flag_hh(c - 2); }
        }
        if ((c == 0 || c == 2) && s == 18 && nh == 0) stage_res(c >> 1);
        if (c < 3) {
          if (nh == 0) {
            if (s >= 2 && s < 18) DMA_K(rws, tvo, (s < 10 ? cX1 : cX2) + (((s - 2) >> 2) & 1) * 4096, ((s - 2) & 3) * 1024, "sc1");
            if (s < 16 && (s & 3) == 1) DMA_M0(tn + wt * 8192 + (s >> 3) * 32768 + (s & 4) * 1024);
          }
          if (s == 25) DMA_M0(L_D0 + w * 2048);
          if (s == 26 || s == 27) DMA_K(rwd, dvo, WD_OFF(c) + (s - 26) * 7168, (s - 26) * 1024, "");
        } else {
          if (s < 18 && (s & 3) == 2) DMA_M0(L_T0 + w * 8192 + (s - 2) * 512);
          if (s == 22) DMA_M0(L_D0 + w * 2048);
          if (s >= 3 && s < 26 && (s & 1) && s != 19 && s != 21) {
            const int k = s < 19 ? (s - 3) >> 1 : (s - 7) >> 1;
            DMA_K(rwd, dvo, WD_OFF(3 + (k >> 1)) + (k & 1) * 7168, (k & 1) * 1024, "");
          }
        }
        if (c > 0 && s == 17) {
          if (c == 3) VMW(8); else if (nh) VMW(0); else VMW(16);
          READ_D(c - 1, c, L_D0 + w * 2048);
        }
        if (c == 0 && s == 6) take_sums();
        if ((c == 1 || c == 3) && s == 15) { if (c == 1) { resa = lds_tr(rra); PIN(resa); } else { resb = lds_tr(rra); PIN(resb); } }
        if (c == 0 && s == 23 && nh == 0) POLL_X1B();
        if (c == 3 && s == 21) POLL_HH(fa, 0);
        if (c == 1) {
          if (s >= 14 && s < 22) epi_b(acc[0], s - 14);
          if (s >= 13 && s < 21) epi_a(acc[0], s - 13);
          if (s == 22) epi_store(0);
        } else if (c > 1) {
          if (s >= 5 && s < 21 && (s & 1)) epi_b(acc[(c - 1) & 1], (s - 5) >> 1);
          if (s >= 4 && s < 20 && !(s & 1)) epi_a(acc[(c - 1) & 1], (s - 4) >> 1);
          if (s == 20) epi_store(c - 1);
        }
        if (s == 21 && c < 3) {
          nX1 = sX1 + TBLK(c + 2 < 4 ? c + 2 : 3) * 65536; nHs = sHs + TBLK(c) * 2 * 112 * 1024;
          nX2 = nX1 ^ 32768;
          PINS(nX1); PINS(nX2); PINS(nHs);
        }
        SB();
      }
      acc[c & 1] = ac;
      Z(3, 1 + 2 * c); Z(8, 4 + 5 * c);
      if (c < 3) VMW(3); else VMW(2);
      if (c == 0) STAMP(15);
      if (c < 3) BAR();
      if (c == 1) STAMP(16);
      Z(3, 2 + 2 * c); Z(8, 5 + 5 * c);
    }
    ZC(3);
    PIN(fa);
    RETRY_HH(0)
    LDH2(0, hh_off(0));
    LDH2(2, hh_off(0));
    stat_check(3);
    stat_take(3);
    stat_fold(3);
    {
      float e8[8];
#pragma unroll
      for (int k = 0; k < 8; k++) e8[k] = __builtin_amdgcn_exp2f(acc[1][8 * (k >> 2) + (k & 3)] * erg);
#pragma unroll
      for (int k = 0; k < 8; k++) eo[k] = acc[1][8 * (k >> 2) + 4 + (k & 3)] * __builtin_amdgcn_rcpf(__builtin_fmaf(e8[k], den, den));
    }
    epi_store(3);
    STAMP(17); Z(4, 1);
    LDH2(4, hh_off(0));
    LDH2(6, hh_off(0));
    VMW(9);
    READ_D1();
    VMW(4);
    Z(4, 2);
    BAR();
    Z(4, 3);
    if (w == 4) { flag_hh(2); flag_hh(3); }
    Z(4, 4); Z(4, 5);
  } else {
    auto ln2_stats = [&](u32 tbase, int c) {
      float s1[2] = {0.f, 0.f}, s2[2] = {0.f, 0.f};
      v2u x[2][8];
#pragma unroll
      for (int nt = 0; nt < 2; nt++)
#pragma unroll
        for (int k = 0; k < 8; k++) x[nt][k] = lds_tr(tbase + (tra ^ (nt * 64)) + w * 8192 + k * 1024);
#pragma unroll
      for (int k = 0; k < 8; k++)
#pragma unroll
        for (int nt = 0; nt < 2; nt++) {
          s1[nt] = dot2(x[nt][k].y, 0x3f803f80u, dot2(x[nt][k].x, 0x3f803f80u, s1[nt]));
          s2[nt] = dot2(x[nt][k].y, x[nt][k].y, dot2(x[nt][k].x, x[nt][k].x, s2[nt]));
        }
#pragma unroll
      for (int nt = 0; nt < 2; nt++) {
        auto sw = __builtin_amdgcn_permlane32_swap(ubits(s1[nt]), ubits(s2[nt]), false, false);
        lds_st<float>(L_STATP + w * 512 + (nt * 32 + (l & 31)) * 8 + (l >> 5) * 4, fbits(sw[0]) + fbits(sw[1]));
      }
      BAR();
      if (w == 0) {
        f2 t[8];
#pragma unroll
        for (int k = 0; k < 8; k++) t[k] = lds_ld<f2>(L_STATP + k * 512 + l * 8);
        const f2 m = (((t[0] + t[1]) + (t[2] + t[3])) + ((t[4] + t[5]) + (t[6] + t[7]))) * (1.f / kD);
        const float var = rbf(fmaxf(m.y - m.x * m.x, 0.f));
        st64_dev(v2u{pk(m.x, __builtin_amdgcn_sqrtf(rbf(var + 1e-5f))), (l & 1) ? tagB : tagA}, rws, l * 8,
                 WS_S2 + ((c >> 1) * 16 + tb0 + (c & 1)) * 512);
      }
    };
    if (flagged) VMW(1); else VMW(0);
    BAR();
    if (g4 < 2) {
      ln2_stats(L_T0, g4);
      stage_t(WS_X1S, 1, L_T1);
    }
    POLL_X1(1);
    VMW(0);
    RETRY_X1(1)
    BAR();
    if (g4 >= 2) stage_t(WS_X1T, g4, L_T0);
    stage_t(WS_X1S, 2, L_T1);
    if (g4 >= 2) {
      VMW(8);
      BAR();
      ln2_stats(L_T0, g4);
    }
    stage_t(WS_X1S, 3, L_T1);
    BAR();
    if (nh == 0) stage_res(0);
    VMW(0);
    BAR();
    resa = lds_tr(rra);
    BAR();
    if (nh == 0) stage_res(1);
    STAGE_D(0, 3, L_T1 + w * 8192);
    STAGE_D(3, 7, L_T0 + w * 8192);
    STAGE_D(7, 8, L_D0 + w * 2048);
    VMW(0);
    BAR();
    resb = lds_tr(rra);
    READ_D(0, 3, L_T1 + w * 8192);
    READ_D1();
    wait_hh(0);
    LDH2(0, hh_off(0)); LDH2(2, hh_off(0)); LDH2(4, hh_off(0)); LDH2(6, hh_off(0));
    BAR();
  }
  STAMP(18);
  PINS(aI); PINS(a4); PINS(aD); PINS(aK); PINS(aR);

#define HF_WAIT(n, s) VMW(n); asm volatile("" : "+v"(hf[s]), "+v"(hf[s + 1])::"memory")
#define MM2(s)                                                                                    \
  acc[0] = mfma32(Wd[s][0], hf[s], acc[0]); acc[1] = mfma32(Wd[s][1], hf[s], acc[1]);             \
  acc[0] = mfma32(Wd[s + 1][0], hf[s + 1], acc[0]); acc[1] = mfma32(Wd[s + 1][1], hf[s + 1], acc[1]); \
  SB()
  auto put_partials = [&](u32 red) {
#pragma unroll
    for (int m2 = 0; m2 < 2; m2++)
#pragma unroll
      for (int g = 0; g < 4; g++)
        lds_st<f4>(red + w * 16384 + (m2 * 4 + g) * 1024 + l16, f4{acc[m2][4 * g], acc[m2][4 * g + 1], acc[m2][4 * g + 2], acc[m2][4 * g + 3]});
  };
  const rsrc_t rout = mk(p.out, kL * kD * 2);
  u32 ora0 = L_X + ora, ora1 = L_X + 65536 + ora, ogb = L_GB + ((w >> 1) * 4 + (l >> 4)) * 16;
  asm volatile("" : "+v"(ora0), "+v"(ora1), "+v"(ogb));
  const u32 d2base = L_X + w * 16384;
  STAGE_D(8, 14, d2base);
  LDH2(8, hh_off(0)); LDH2(10, hh_off(0)); LDH2(12, hh_off(0));
  STAMP(19); Z(4, 6);
  acc[0] = acc[1] = f16v{};
  VMW(18);
  asm volatile("" : "+v"(hf[0]), "+v"(hf[1]), "+v"(hf[2]), "+v"(hf[3]), "+v"(hf[4]), "+v"(hf[5]), "+v"(hf[6]), "+v"(hf[7])::"memory");
  MM2(0); MM2(2); MM2(4); MM2(6);
  Z(4, 7);
  POLL_HH(fa, 1);
  VMW(0);
  asm volatile("" : "+v"(hf[8]), "+v"(hf[9]), "+v"(hf[10]), "+v"(hf[11]), "+v"(hf[12]), "+v"(hf[13]), "+v"(fa)::"memory");
  Z(4, 8);
  READ_D(8, 14, d2base);
  RETRY_HH(1)
  Z(4, 9);
  const u32 sob = hh_off(1);
  LDH2(0, sob); LDH2(2, sob); LDH2(4, sob); LDH2(6, sob);
  SB();
  MM2(8);
  LDH2(8, sob);
  MM2(10);
  LDH2(10, sob);
  MM2(12);
  LDH2(12, sob);
  Z(4, 10);
  put_partials(L_X);
  STAMP(20); Z(4, 11);
  acc[0] = acc[1] = f16v{};
  HF_WAIT(12, 0); MM2(0);
  HF_WAIT(10, 2); MM2(2);
  HF_WAIT(8, 4); MM2(4);
  HF_WAIT(6, 6); MM2(6);
  HF_WAIT(4, 8); MM2(8);
  HF_WAIT(2, 10); MM2(10);
  HF_WAIT(0, 12); MM2(12);
  Z(4, 14);
  put_partials(L_X + 8192);
  STAMP(21); Z(4, 15);
  BAR();
  Z(4, 16);
  {
    f4 t[2][8];
#pragma unroll
    for (int h = 0; h < 2; h++)
#pragma unroll
      for (int k = 0; k < 8; k++) t[h][k] = lds_ld<f4>((k < 4 ? ora0 : ora1) + (k & 3) * 16384 + h * 8192);
    const f4 gb = lds_ld<f4>(ogb);
    SB();
#pragma unroll
    for (int h = 0; h < 2; h++) {
      const f4 z = ((t[h][0] + t[h][1]) + (t[h][2] + t[h][3])) + ((t[h][4] + t[h][5]) + (t[h][6] + t[h][7]));
      const v2u res = h ? resb : resa;
      const v2u o = {pk2(fma2(gb.xy, z.xy, up2(res.x))), pk2(fma2(gb.zw, z.zw, up2(res.y)))};
      asm volatile("buffer_store_dwordx2 %0, %1, %2, %3 offen" ::"v"(o), "v"(ovo), "s"(rout), "s"(sOut + h * 1024 * 1024) : "memory");
    }
  }
  STAMP(22); ZC(4);
}

}

extern "C" void kernel_launch(void* const* d_in, const int* in_sizes, int n_in, void* d_out, int out_size, void* d_ws,
                              size_t ws_size, hipStream_t stream) {
  using namespace s4glu;
  (void)in_sizes; (void)n_in; (void)out_size; (void)ws_size;
  Params p;
  p.x = d_in[0]; p.are = d_in[1]; p.aim = d_in[2]; p.bre = d_in[3]; p.bim = d_in[4]; p.cre = d_in[5]; p.cim = d_in[6];
  p.logdt = d_in[7]; p.dskip = d_in[8]; p.wg = d_in[10]; p.wu = d_in[11]; p.wd = d_in[12]; p.c = d_in[13]; p.wmod = d_in[14];
  p.out = d_out; p.ws = d_ws;
  (void)hipFuncSetAttribute((const void*)fused_kernel, hipFuncAttributeMaxDynamicSharedMemorySize, L_END);
  fused_kernel<<<256, 512, L_END, stream>>>(p);
}
```
